```python
import torch

def _softmax(x, axis=-1):
    return torch.softmax(x, dim=axis)
def _gelu(x):
    return torch.nn.functional.gelu(x, approximate="tanh")
def _silu(x):
    return torch.nn.functional.silu(x)
def _sigmoid(x):
    return torch.sigmoid(x)
def _layernorm(x, axis=-1, eps=1e-5):
    m = x.mean(dim=axis, keepdim=True)
    v = x.var(dim=axis, keepdim=True, unbiased=False)
    return (x - m) / torch.sqrt(v + eps)
def _scatter_add(tgt, idx, src, axis=0):
    return tgt.clone().index_add_(axis, idx, src)
def _segment_sum(src, seg, n):
    out = torch.zeros((n,) + src.shape[1:], dtype=src.dtype)
    return out.index_add_(0, seg, src)
def _pad(x, pw):
    flat = tuple(v for pair in reversed(pw) for v in pair)
    return torch.nn.functional.pad(x, flat)

kernel_name = 'concat_heads__ssm_conv_h3__softmax_full_mha__prompt'

def setup_inputs():

    _w = (lambda a: (lambda t: t.to(torch.bfloat16) if t.is_floating_point() else t)(torch.as_tensor(a)))
    def _A_setup():
        B, S, D = 1, 2048, 512
        ks = 4
        return {
            "q": _w((lambda *s: torch.randn(*s, dtype=torch.bfloat16) * 0.05)(B, S, D)),
            "k": _w((lambda *s: torch.randn(*s, dtype=torch.bfloat16) * 0.4)(B, S, D)),
            "v": _w((lambda *s: torch.randn(*s, dtype=torch.bfloat16) * 0.4)(B, S, D)),
            "shift_k": _w((lambda *s: torch.randn(*s, dtype=torch.bfloat16) * 0.4)(D, ks) * 0.5),
            "ssm_k": _w((lambda *s: torch.randn(*s, dtype=torch.bfloat16) * 1.6)(D, S) * (1.0 / S ** 0.5)),
            "ssm_D": _w((lambda *s: torch.randn(*s, dtype=torch.bfloat16) * 0.4)(D)),
        }
    _A_in = _A_setup()

    def _B_setup():
        B, S, D = 1, 2048, 512
        return {
            "x": _w((lambda *s: torch.randn(*s, dtype=torch.bfloat16) * 0.2)(B, S, D)),
            "wqkv": _w((lambda *s: torch.randn(*s, dtype=torch.bfloat16) * 0.2)(D, 3 * D)),
            "wo": _w((lambda *s: torch.randn(*s, dtype=torch.bfloat16) * 0.05)(D, D)),
        }
    _B_in = _B_setup()

    return {
        "A_q": _A_in["q"],
        "A_k": _A_in["k"],
        "A_v": _A_in["v"],
        "A_shift_k": _A_in["shift_k"],
        "A_ssm_k": _A_in["ssm_k"],
        "A_ssm_D": _A_in["ssm_D"],
        "B_x": _B_in["x"],
        "B_wqkv": _B_in["wqkv"],
        "B_wo": _B_in["wo"],
    }

def reference(A_q, A_k, A_v, A_shift_k, A_ssm_k, A_ssm_D, B_x, B_wqkv, B_wo):
    def _A_ref(q, k, v, shift_k, ssm_k, ssm_D):
        B, S, D = q.shape
        ks = shift_k.shape[-1]
        k_pad = _pad(k, ((0, 0), (ks - 1, 0), (0, 0)))
        k_shift = torch.zeros_like(k)
        for t in range(ks):
            k_shift = k_shift + k_pad[:, t:t + S, :] * shift_k[:, ks - 1 - t][None, None, :]
        kv = k_shift * v
        L = 2 * S
        kv_pad = _pad(kv, ((0, 0), (0, S), (0, 0)))
        kf_pad = _pad(ssm_k, ((0, 0), (0, S)))
        kv_f = (lambda x, axes=(-2,-1): torch.fft.rfft2(x.float(), dim=axes))(kv_pad[:, None, :, :], axes=(1, 2))
        kf_f = (lambda x, axes=(-2,-1): torch.fft.rfft2(x.float(), dim=axes))(kf_pad[:, None, :], axes=(1, 2))
        kf_f = (lambda x, *d: x.permute(*d))(kf_f, 1, 2, 0)
        y_f = kv_f * kf_f[None, :, :, :]
        y = (lambda x, s, axes=(-2,-1): torch.fft.irfft2(x, s=s, dim=axes).to(torch.bfloat16))(y_f, (1, L), axes=(1, 2))[:, 0, :S, :]
        y = y + kv * ssm_D[None, None, :]
        return q * y

    def _B_ref(x, wqkv, wo):
        B, S, D = x.shape
        H = 32; Dh = D // H
        qkv = (lambda x, *d: x.permute(*d))((x @ wqkv).reshape(B, S, 3, H, Dh), 2, 0, 3, 1, 4)
        q, k, v = qkv[0], qkv[1], qkv[2]
        scale = Dh ** -0.5
        scores = torch.einsum("bhqd,bhkd->bhqk", q, k) * scale
        p = _softmax(scores, axis=-1)
        o = torch.einsum("bhqk,bhkd->bhqd", p, v)
        return (lambda x, *d: x.permute(*d))(o, 0, 2, 1, 3).reshape(B, S, D) @ wo

    a = _A_ref(A_q, A_k, A_v, A_shift_k, A_ssm_k, A_ssm_D)
    b = _B_ref(B_x, B_wqkv, B_wo)
    return torch.concatenate([a, b], axis=-1)
```

```cpp
#include <hip/hip_runtime.h>
#include <hip/hip_bf16.h>
#include <stdint.h>

typedef __bf16 bf16x8 __attribute__((ext_vector_type(8)));
typedef __bf16 bf16x16 __attribute__((ext_vector_type(16)));
typedef short s16x4 __attribute__((ext_vector_type(4)));
typedef float f32x16 __attribute__((ext_vector_type(16)));
typedef float f32x8 __attribute__((ext_vector_type(8)));
typedef float f32x4 __attribute__((ext_vector_type(4)));
typedef unsigned u32x4 __attribute__((ext_vector_type(4)));
typedef unsigned u32x2 __attribute__((ext_vector_type(2)));
typedef unsigned short u16;
#define LAS __attribute__((address_space(3)))
#define GAS __attribute__((address_space(1)))

constexpr int S = 2048, D = 512, NH = 32;
constexpr float QSCALE = 0.25f * 1.4426950408889634f;
constexpr float FAST_BOUND2 = 60.f * 60.f;

constexpr size_t WS_QF = 0, WS_KF = 2u << 20, WS_VF = 4u << 20, WS_OF = 6u << 20, WS_UR = 8u << 20, WS_ZT = 10u << 20,
                 WS_KN = 12u << 20, WS_QN = (12u << 20) + 8192;

#define MFMA32(a, b, c) __builtin_amdgcn_mfma_f32_32x32x16_bf16(a, b, c, 0, 0, 0)
#define BAR() asm volatile("s_barrier" ::: "memory")
#define WAITV(n) asm volatile("s_waitcnt vmcnt(" #n ")" ::: "memory")
#define WAITL0() asm volatile("s_waitcnt lgkmcnt(0)" ::: "memory")

__device__ __forceinline__ unsigned cvtpk(float lo, float hi) {
    typedef __bf16 bf2 __attribute__((ext_vector_type(2)));
    bf2 r; r[0] = (__bf16)lo; r[1] = (__bf16)hi; return __builtin_bit_cast(unsigned, r);
}
__device__ __forceinline__ float bflo(unsigned u) { return __uint_as_float(u << 16); }
__device__ __forceinline__ float bfhi(unsigned u) { return __uint_as_float(u & 0xffff0000u); }
__device__ __forceinline__ float rbf(float f) { return __uint_as_float(cvtpk(f, 0.f) << 16); }
__device__ __forceinline__ void glds16(const void* g, LAS void* l) {
    __builtin_amdgcn_global_load_lds((const GAS unsigned*)g, (LAS unsigned*)l, 16, 0, 0);
}
__device__ __forceinline__ s16x4 trread(LAS const char* p) {
    typedef short v4i16_t __attribute__((ext_vector_type(4)));
    return __builtin_amdgcn_ds_read_tr16_b64_v4i16((LAS v4i16_t*)p);
}
__device__ __forceinline__ bf16x8 cat8(s16x4 a, s16x4 b) {
    typedef short s16x8 __attribute__((ext_vector_type(8)));
    s16x8 r = __builtin_shufflevector(a, b, 0, 1, 2, 3, 4, 5, 6, 7);
    return __builtin_bit_cast(bf16x8, r);
}
__device__ __forceinline__ f32x16 smf(bf16x8 a, f32x8 b, f32x16 c) {
    return __builtin_amdgcn_smfmac_f32_32x32x32_bf16(a, __builtin_bit_cast(bf16x16, b), c, 0xDDDD, 0, 0);
}
__device__ __forceinline__ f32x8 lo8(f32x16 x) { return __builtin_shufflevector(x, x, 0, 1, 2, 3, 4, 5, 6, 7); }
__device__ __forceinline__ f32x8 hi8(f32x16 x) { return __builtin_shufflevector(x, x, 8, 9, 10, 11, 12, 13, 14, 15); }
__device__ __forceinline__ float swap32(float x) {
    auto r = __builtin_amdgcn_permlane32_swap(__float_as_uint(x), __float_as_uint(x), false, false);
    return __uint_as_float((threadIdx.x & 32) ? r[0] : r[1]);
}

constexpr int K1_STAGE = 28672, K1_NST = 5, K1_LDS = K1_STAGE * K1_NST;

__global__ __launch_bounds__(512, 2) void k1_qkv(const u16* __restrict__ x, const u16* __restrict__ w, const u16* __restrict__ kin,
                                                  const u16* __restrict__ vin, const u16* __restrict__ shk, char* __restrict__ ws) {
    extern __shared__ __attribute__((aligned(16))) char smem[];
    LAS char* lds = (LAS char*)smem;
    const int tid = threadIdx.x, lane = tid & 63;
    const int wid = __builtin_amdgcn_readfirstlane(tid >> 6);
    const int bid = blockIdx.x, xc = bid & 7, jj = bid >> 3;
    const int rb = (xc & 3) * 4 + (jj & 3), hp = (xc >> 2) * 8 + (jj >> 2);
    if (wid >= 4) {
        const int pw = wid - 4;
        const int slab = bid >> 2, cg = bid & 3;
        const int s0 = slab * 32 + pw * 8, ch = cg * 128 + lane * 2;
        unsigned kk[11], vv[8];
#pragma unroll
        for (int i = 0; i < 11; ++i) { const int s = s0 - 3 + i; kk[i] = (s >= 0) ? *(const unsigned*)(kin + (size_t)s * D + ch) : 0u; }
#pragma unroll
        for (int i = 0; i < 8; ++i) vv[i] = *(const unsigned*)(vin + (size_t)(s0 + i) * D + ch);
        const u32x4 sw = *(const u32x4*)(shk + ch * 4);
        const u16* xsrc[4];
#pragma unroll
        for (int i = 0; i < 4; ++i) { const int m = pw * 4 + i, row = 8 * m + (lane >> 3), c = (lane & 7) ^ ((row >> 1) & 7);
            xsrc[i] = x + (size_t)(rb * 128 + row) * D + c * 8; }
        const u16* wsrc = w + (size_t)(16 * pw + (lane >> 2)) * (3 * D) + hp * 32 + (lane & 3) * 8;
        auto issue = [&](int c) {
            LAS char* st = lds + (c % K1_NST) * K1_STAGE;
#pragma unroll
            for (int i = 0; i < 4; ++i) glds16(xsrc[i] + c * 64, st + (pw * 4 + i) * 1024);
#pragma unroll
            for (int g = 0; g < 3; ++g) glds16(wsrc + (size_t)c * 64 * (3 * D) + g * D, st + 16384 + g * 4096 + pw * 1024);
        };
        issue(0); issue(1); issue(2); issue(3); issue(4);
        WAITV(28); BAR();
        WAITV(21); BAR(); issue(5);
        WAITV(21); BAR(); issue(6);
        WAITV(21); BAR(); issue(7);
        WAITV(21); BAR();
        WAITV(14); BAR();
        WAITV(7); BAR();
        WAITV(0); BAR();
        float w0[4], w1[4];
        w0[0] = bflo(sw[0]); w0[1] = bfhi(sw[0]); w0[2] = bflo(sw[1]); w0[3] = bfhi(sw[1]);
        w1[0] = bflo(sw[2]); w1[1] = bfhi(sw[2]); w1[2] = bflo(sw[3]); w1[3] = bfhi(sw[3]);
        float r0[8], r1[8];
#pragma unroll
        for (int i = 0; i < 8; ++i) {
            float a0 = bflo(kk[i]) * w0[3], a1 = bfhi(kk[i]) * w1[3];
            a0 += bflo(kk[i + 1]) * w0[2]; a1 += bfhi(kk[i + 1]) * w1[2];
            a0 += bflo(kk[i + 2]) * w0[1]; a1 += bfhi(kk[i + 2]) * w1[1];
            a0 += bflo(kk[i + 3]) * w0[0]; a1 += bfhi(kk[i + 3]) * w1[0];
            r0[i] = rbf(a0) * bflo(vv[i]); r1[i] = rbf(a1) * bfhi(vv[i]);
        }
        u16* ur = (u16*)(ws + WS_UR);
        const int xo = S - 8 - s0;
        u32x4 o0 = {cvtpk(r0[7], r0[6]), cvtpk(r0[5], r0[4]), cvtpk(r0[3], r0[2]), cvtpk(r0[1], r0[0])};
        u32x4 o1 = {cvtpk(r1[7], r1[6]), cvtpk(r1[5], r1[4]), cvtpk(r1[3], r1[2]), cvtpk(r1[1], r1[0])};
        *(u32x4*)(ur + (size_t)ch * S + xo) = o0;
        *(u32x4*)(ur + (size_t)(ch + 1) * S + xo) = o1;
        return;
    }
    const int i32 = lane & 31, hh = lane >> 5;
    const int row = wid * 32 + i32, swz = (row >> 1) & 7;
    int xoff[4];
#pragma unroll
    for (int s = 0; s < 4; ++s) xoff[s] = row * 128 + (((2 * s + hh) ^ swz) * 16);
    const int woff = 16384 + (8 * hh + ((lane & 15) >> 2)) * 64 + ((lane >> 4) & 1) * 32 + (lane & 3) * 8;
    f32x16 aq = {}, ak = {}, av = {};
    for (int c = 0; c < 8; ++c) {
        BAR();
        LAS const char* st = lds + (c % K1_NST) * K1_STAGE;
#pragma unroll
        for (int s = 0; s < 4; ++s) {
            const bf16x8 xf = *(LAS const bf16x8*)(st + xoff[s]);
            LAS const char* wp = st + woff + s * 1024;
            const bf16x8 wq = cat8(trread(wp), trread(wp + 256));
            const bf16x8 wk = cat8(trread(wp + 4096), trread(wp + 4096 + 256));
            const bf16x8 wv = cat8(trread(wp + 8192), trread(wp + 8192 + 256));
            aq = MFMA32(wq, xf, aq);
            ak = MFMA32(wk, xf, ak);
            av = MFMA32(xf, wv, av);
        }
        WAITL0();
    }
    const int tt = rb * 4 + wid;
    const int h0 = hp * 2;
    {
        char* qf = ws + WS_QF; char* kf = ws + WS_KF;
        const int pi = (i32 & 19) | ((i32 & 4) << 1) | ((i32 & 8) >> 1);
        float nq[2], nk[2];
#pragma unroll
        for (int e = 0; e < 2; ++e) {
            float q8[8], k8[8]; float sq = 0.f, sk = 0.f;
#pragma unroll
            for (int j = 0; j < 8; ++j) { q8[j] = aq[8 * e + j] * QSCALE; k8[j] = ak[8 * e + j]; sq += q8[j] * q8[j]; sk += k8[j] * k8[j]; }
            u32x4 qv = {cvtpk(q8[0], q8[1]), cvtpk(q8[2], q8[3]), cvtpk(q8[4], q8[5]), cvtpk(q8[6], q8[7])};
            u32x4 kv = {cvtpk(k8[0], k8[1]), cvtpk(k8[2], k8[3]), cvtpk(k8[4], k8[5]), cvtpk(k8[6], k8[7])};
            *(u32x4*)(qf + ((size_t)((h0 + e) * 64 + tt) * 64 + i32 + 32 * hh) * 16) = qv;
            *(u32x4*)(kf + ((size_t)((h0 + e) * 64 + tt) * 64 + pi + 32 * hh) * 16) = kv;
            nq[e] = sq; nk[e] = sk;
        }
#pragma unroll
        for (int e = 0; e < 2; ++e) {
            float a = nq[e] + __shfl_xor(nq[e], 32), b = nk[e] + __shfl_xor(nk[e], 32);
#pragma unroll
            for (int m = 1; m < 32; m <<= 1) { const float a2 = __shfl_xor(a, m), b2 = __shfl_xor(b, m);
                a = (a2 > a || a2 != a2) ? a2 : a; b = (b2 > b || b2 != b2) ? b2 : b; }
            if (lane == 0) { ((float*)(ws + WS_QN))[(h0 + e) * 64 + tt] = a; ((float*)(ws + WS_KN))[(h0 + e) * 64 + tt] = b; }
        }
    }
    {
        char* vf = ws + WS_VF;
        const int e = i32 >> 4, d = i32 & 15;
#pragma unroll
        for (int sub = 0; sub < 2; ++sub) {
            u32x4 vv = {cvtpk(av[8 * sub + 0], av[8 * sub + 1]), cvtpk(av[8 * sub + 2], av[8 * sub + 3]), cvtpk(av[8 * sub + 4], av[8 * sub + 5]), cvtpk(av[8 * sub + 6], av[8 * sub + 7])};
            *(u32x4*)(vf + ((size_t)(((h0 + e) * 64 + tt) * 2 + sub)) * 512 + hh * 256 + d * 16) = vv;
        }
    }
}

constexpr int AT_K = 0, AT_V = 65536, AT_ONES = 131072, AT_LDS = 131072 + 2048;

__global__ __launch_bounds__(512, 2) void k2_attn(char* __restrict__ ws) {
    extern __shared__ __attribute__((aligned(16))) char smem[];
    LAS char* lds = (LAS char*)smem;
    const int tid = threadIdx.x, lane = tid & 63;
    const int wid = __builtin_amdgcn_readfirstlane(tid >> 6);
    const int bid = blockIdx.x, xc = bid & 7, jj = bid >> 3;
    const int head = xc * 4 + (jj >> 3), qb = jj & 7;
    const int qt = qb * 8 + wid;
    const char* ksrc = ws + WS_KF + (size_t)head * 65536 + wid * 1024 + lane * 16;
    const char* vsrc = ws + WS_VF + (size_t)head * 65536 + wid * 1024 + lane * 16;
#pragma unroll
    for (int c = 0; c < 8; ++c) { glds16(ksrc + c * 8192, lds + AT_K + c * 8192 + wid * 1024); glds16(vsrc + c * 8192, lds + AT_V + c * 8192 + wid * 1024); }
    const bf16x8 qf = *(const bf16x8*)(ws + WS_QF + ((size_t)(head * 64 + qt) * 64 + lane) * 16);
    float kn = ((const float*)(ws + WS_KN))[head * 64 + lane];
    float qn = ((const float*)(ws + WS_QN))[head * 64 + qb * 8 + (lane & 7)];
#pragma unroll
    for (int m = 1; m < 64; m <<= 1) { const float a = __shfl_xor(kn, m), b = __shfl_xor(qn, m); kn = (a > kn || a != a) ? a : kn; qn = (b > qn || b != b) ? b : qn; }
    const bool fast = __builtin_amdgcn_readfirstlane((int)(kn * qn * 1.02f <= FAST_BOUND2)) != 0;
    if (tid < 128) *(LAS u32x4*)(lds + AT_ONES + tid * 16) = u32x4{0x3F803F80u, 0x3F803F80u, 0x3F803F80u, 0x3F803F80u};
    const int i32 = lane & 31, hh = lane >> 5;
    const bool real = i32 < 16;
    const int cpos = (i32 >= 20 && i32 < 28) ? 64 : 0;
    int vaddr = real ? (AT_V + hh * 256 + i32 * 16) : (AT_ONES + cpos);
    const int vstep = real ? 2048 : 0;
    f32x16 o = {};
    WAITV(0); WAITL0(); BAR();
    if (fast) {
        for (int kt = 0; kt < 64; kt += 2) {
#pragma unroll
            for (int u = 0; u < 2; ++u) {
                const bf16x8 kf = *(LAS const bf16x8*)(lds + AT_K + (kt + u) * 1024 + lane * 16);
                const bf16x8 v0 = *(LAS const bf16x8*)(lds + vaddr + u * 1024);
                const bf16x8 v1 = *(LAS const bf16x8*)(lds + vaddr + u * 1024 + 512);
                f32x16 s = MFMA32(kf, qf, (f32x16){});
#pragma unroll
                for (int r = 0; r < 16; ++r) s[r] = __builtin_amdgcn_exp2f(s[r]);
                o = smf(v0, lo8(s), o);
                o = smf(v1, hi8(s), o);
            }
            vaddr += vstep;
        }
    } else {
        float m = -INFINITY;
        for (int kt = 0; kt < 64; kt += 2) {
#pragma unroll
            for (int u = 0; u < 2; ++u) {
                const bf16x8 kf = *(LAS const bf16x8*)(lds + AT_K + (kt + u) * 1024 + lane * 16);
                const bf16x8 v0 = *(LAS const bf16x8*)(lds + vaddr + u * 1024);
                const bf16x8 v1 = *(LAS const bf16x8*)(lds + vaddr + u * 1024 + 512);
                f32x16 s = MFMA32(kf, qf, (f32x16){});
                float tm = s[0];
#pragma unroll
                for (int r = 1; r < 16; ++r) tm = (s[r] > tm || s[r] != s[r]) ? s[r] : tm;
                { const float t2 = swap32(tm); tm = (t2 > tm || t2 != t2) ? t2 : tm; }
                const float mn = (tm > m || tm != tm) ? tm : m;
                const float ms = (mn == -INFINITY) ? 0.f : mn;
                const float alpha = __builtin_amdgcn_exp2f(m - ms);
                m = mn;
#pragma unroll
                for (int r = 0; r < 16; ++r) { o[r] *= alpha; s[r] = __builtin_amdgcn_exp2f(s[r] - ms); }
                o = smf(v0, lo8(s), o);
                o = smf(v1, hi8(s), o);
            }
            vaddr += vstep;
        }
    }
    const float inv = 1.0f / o[8];
    u32x4 ov = {cvtpk(o[0] * inv, o[1] * inv), cvtpk(o[2] * inv, o[3] * inv), cvtpk(o[4] * inv, o[5] * inv), cvtpk(o[6] * inv, o[7] * inv)};
    *(u32x4*)(ws + WS_OF + ((size_t)(qt * 32 + head) * 64 + lane) * 16) = ov;
}

constexpr int CV_H = 0, CV_HB = 8320, CV_U = 2 * CV_HB, CV_UB = 95 * 72 + 8  , CV_LDS = CV_U + 2 * CV_UB;

__global__ __launch_bounds__(256) void k2_conv(const u16* __restrict__ ssk, const u16* __restrict__ ssd, char* __restrict__ ws) {
    extern __shared__ __attribute__((aligned(16))) char smem[];
    LAS char* lds = (LAS char*)smem;
    const int tid = threadIdx.x, lane = tid & 63;
    const int wid = __builtin_amdgcn_readfirstlane(tid >> 6);
    const int c2 = wid >> 1, tile = wid & 1, ch = blockIdx.x * 2 + c2;
    LAS char* hb = lds + CV_H + c2 * CV_HB;
    LAS char* ub = lds + CV_U + c2 * CV_UB;
    const int t128 = tid & 127;
    {
        const u16* hrow = ssk + (size_t)ch * S;
        const u16* urow = (const u16*)(ws + WS_UR) + (size_t)ch * S;
#pragma unroll
        for (int i = 0; i < 2; ++i) {
            const int t = t128 + 128 * i;
            const u32x4 hv = *(const u32x4*)(hrow + 8 * t);
            const unsigned prev = t > 0 ? (unsigned)hrow[8 * t - 1] : 0u;
            *(LAS u32x4*)(hb + (16 + 4 * t) * 4) = hv;
            u32x4 sv = {(hv[0] << 16) | prev, __builtin_amdgcn_alignbit(hv[1], hv[0], 16), __builtin_amdgcn_alignbit(hv[2], hv[1], 16), __builtin_amdgcn_alignbit(hv[3], hv[2], 16)};
            *(LAS u32x4*)(hb + 4160 + (16 + 4 * t) * 4) = sv;
            const u32x4 uv = *(const u32x4*)(urow + 8 * t);
            LAS char* up = ub + (t >> 2) * 72 + (t & 3) * 16;
            *(LAS u32x2*)(up) = u32x2{uv[0], uv[1]}; *(LAS u32x2*)(up + 8) = u32x2{uv[2], uv[3]};
        }
        if (t128 < 16) { *(LAS unsigned*)(hb + t128 * 4) = 0u; *(LAS unsigned*)(hb + 4160 + t128 * 4) = 0u; }
        for (int i = t128; i < 31 * 9; i += 128) *(LAS u32x2*)(ub + 64 * 72 + i * 8) = u32x2{0u, 0u};
    }
    __syncthreads();
    const int r = lane & 31, hh = lane >> 5;
    const int t0 = tile * 1024;
    LAS const char* ap = hb + ((r & 1) ? 0 : 4160) + (((r + 2) >> 1) + 4 * hh) * 4;
    LAS const char* bp = ub + (63 - t0 / 32 - r) * 72 + 16 * hh;
    f32x16 acc = {};
    const int nst = (t0 + 1024) / 32;
    for (int n2 = 0; n2 < nst; ++n2) {
#pragma unroll
        for (int u = 0; u < 2; ++u) {
            LAS const unsigned* a4 = (LAS const unsigned*)(ap + n2 * 64 + u * 32);
            u32x4 av = {a4[0], a4[1], a4[2], a4[3]};
            LAS const char* b = bp + n2 * 72 + u * 32;
            const u32x2 b0 = *(LAS const u32x2*)b, b1 = *(LAS const u32x2*)(b + 8);
            u32x4 bv = {b0[0], b0[1], b1[0], b1[1]};
            acc = MFMA32(__builtin_bit_cast(bf16x8, av), __builtin_bit_cast(bf16x8, bv), acc);
        }
    }
    const float dd = bflo((unsigned)ssd[ch]);
    u16* zt = (u16*)(ws + WS_ZT) + (size_t)ch * S + t0 + 32 * r;
    LAS const char* ue = ub + (63 - t0 / 32 - r) * 72;
#pragma unroll
    for (int g = 0; g < 4; ++g) {
        const int rr = 8 * g + 4 * hh;
        const u32x2 uu = *(LAS const u32x2*)(ue + (28 - rr) * 2);
        const float u3 = bflo(uu[0]), u2 = bfhi(uu[0]), u1 = bflo(uu[1]), u0 = bfhi(uu[1]);
        const float z0 = rbf(acc[4 * g + 0]) + rbf(u0 * dd), z1 = rbf(acc[4 * g + 1]) + rbf(u1 * dd);
        const float z2 = rbf(acc[4 * g + 2]) + rbf(u2 * dd), z3 = rbf(acc[4 * g + 3]) + rbf(u3 * dd);
        *(u32x2*)(zt + rr) = u32x2{cvtpk(z0, z1), cvtpk(z2, z3)};
    }
}

constexpr int K3_LDS = 65536;
__global__ __launch_bounds__(256) void k3_out(const u16* __restrict__ wo, const u16* __restrict__ qin, const char* __restrict__ ws, u16* __restrict__ out) {
    extern __shared__ __attribute__((aligned(16))) char smem[];
    LAS char* lds = (LAS char*)smem;
    const int tid = threadIdx.x, lane = tid & 63;
    const int wid = __builtin_amdgcn_readfirstlane(tid >> 6);
    const int bid = blockIdx.x;
    if (bid < 256) {
        const int xc = bid & 7, jj = bid >> 3;
        const int rt = xc * 4 + (jj >> 3), ct = jj & 7;
#pragma unroll
        for (int i = 0; i < 16; ++i) {
            const int m = wid * 16 + i, half = m >> 5, k = (m & 31) * 16 + (lane >> 2);
            glds16(wo + (size_t)k * D + ct * 64 + half * 32 + (lane & 3) * 8, lds + m * 1024);
        }
        const int wr = wid >> 1, wc = wid & 1, qt = rt * 2 + wr;
        const char* of = ws + WS_OF + ((size_t)qt * 32 * 64 + lane) * 16;
        bf16x8 af[32];
#pragma unroll
        for (int h = 0; h < 32; ++h) af[h] = *(const bf16x8*)(of + h * 1024);
        WAITV(0); BAR();
        const int hh = lane >> 5;
        LAS const char* wp = lds + wc * 32768 + (4 * hh + ((lane & 15) >> 2)) * 64 + ((lane >> 4) & 1) * 32 + (lane & 3) * 8;
        f32x16 acc = {};
#pragma unroll
        for (int h = 0; h < 32; ++h) {
            const bf16x8 bf = cat8(trread(wp + h * 1024), trread(wp + h * 1024 + 512));
            acc = MFMA32(af[h], bf, acc);
        }
        u16* op = out + (size_t)(qt * 32) * (2 * D) + D + ct * 64 + wc * 32 + (lane & 31);
#pragma unroll
        for (int r = 0; r < 16; ++r) { const int row = (r & 3) + 8 * (r >> 2) + 4 * hh; op[(size_t)row * (2 * D)] = (u16)(cvtpk(acc[r], 0.f) & 0xffffu); }
    } else {
        const int t = bid - 256, st = t >> 3, dt = t & 7;
        const u16* zt = (const u16*)(ws + WS_ZT);
        LAS u16* tl = (LAS u16*)lds;
#pragma unroll
        for (int i = 0; i < 2; ++i) { const int p = tid + 256 * i, d = p >> 3, c = p & 7;
            const u32x4 z = *(const u32x4*)(zt + (size_t)(dt * 64 + d) * S + st * 64 + c * 8);
            *(LAS u32x4*)(tl + d * 72 + c * 8) = z; }
        __syncthreads();
#pragma unroll
        for (int i = 0; i < 2; ++i) { const int p = tid + 256 * i, s = p >> 3, c = p & 7;
            const u32x4 qv = *(const u32x4*)(qin + (size_t)(st * 64 + s) * D + dt * 64 + c * 8);
            u32x4 ov;
#pragma unroll
            for (int j = 0; j < 4; ++j) { const float z0 = bflo((unsigned)tl[(c * 8 + 2 * j) * 72 + s]), z1 = bflo((unsigned)tl[(c * 8 + 2 * j + 1) * 72 + s]);
                ov[j] = cvtpk(bflo(qv[j]) * z0, bfhi(qv[j]) * z1); }
            *(u32x4*)(out + (size_t)(st * 64 + s) * (2 * D) + dt * 64 + c * 8) = ov; }
    }
}

extern "C" void kernel_launch(void* const* d_in, const int* in_sizes, int n_in, void* d_out, int out_size, void* d_ws, size_t ws_size, hipStream_t stream) {
    const u16* q = (const u16*)d_in[0]; const u16* k = (const u16*)d_in[1]; const u16* v = (const u16*)d_in[2];
    const u16* shk = (const u16*)d_in[3]; const u16* ssk = (const u16*)d_in[4]; const u16* ssd = (const u16*)d_in[5];
    const u16* x = (const u16*)d_in[6]; const u16* wqkv = (const u16*)d_in[7]; const u16* wo = (const u16*)d_in[8];
    char* ws = (char*)d_ws;
    (void)hipFuncSetAttribute((const void*)k1_qkv, hipFuncAttributeMaxDynamicSharedMemorySize, K1_LDS);
    (void)hipFuncSetAttribute((const void*)k2_attn, hipFuncAttributeMaxDynamicSharedMemorySize, AT_LDS);
    (void)hipFuncSetAttribute((const void*)k2_conv, hipFuncAttributeMaxDynamicSharedMemorySize, CV_LDS);
    (void)hipFuncSetAttribute((const void*)k3_out, hipFuncAttributeMaxDynamicSharedMemorySize, K3_LDS);
    k1_qkv<<<256, 512, K1_LDS, stream>>>(x, wqkv, k, v, shk, ws);
    k2_attn<<<256, 512, AT_LDS, stream>>>(ws);
    k2_conv<<<256, 256, CV_LDS, stream>>>(ssk, ssd, ws);
    k3_out<<<256 + 256, 256, K3_LDS, stream>>>(wo, q, ws, (u16*)d_out);
}
```

```cpp
#include <hip/hip_runtime.h>
#include <hip/hip_bf16.h>
#include <stdint.h>

typedef __bf16 bf16x8 __attribute__((ext_vector_type(8)));
typedef __bf16 bf16x16 __attribute__((ext_vector_type(16)));
typedef short s16x4 __attribute__((ext_vector_type(4)));
typedef float f32x16 __attribute__((ext_vector_type(16)));
typedef float f32x8 __attribute__((ext_vector_type(8)));
typedef float f32x4 __attribute__((ext_vector_type(4)));
typedef unsigned u32x4 __attribute__((ext_vector_type(4)));
typedef unsigned u32x2 __attribute__((ext_vector_type(2)));
typedef unsigned short u16;
#define LAS __attribute__((address_space(3)))
#define GAS __attribute__((address_space(1)))
typedef __amdgpu_buffer_rsrc_t srd_t;

constexpr int S = 2048, D = 512;
constexpr float QSCALE = 0.25f * 1.4426950408889634f;
constexpr float FAST_BOUND2 = 60.f * 60.f;

constexpr unsigned WS_QF = 0, WS_KF = 2u << 20, WS_VF = 4u << 20, WS_OF = 6u << 20, WS_UR = 8u << 20, WS_ZT = 10u << 20,
                   WS_F1A = 12u << 20,
                   WS_F1B = WS_F1A + 16384,
                   WS_F2A = WS_F1B + 2048,
                   WS_F2B = WS_F2A + 2048,
                   WS_EPOCH = WS_F2B + 2048,
                   WS_BYTES = 13u << 20;
#define FUSED 1
#define CONV_PRIO 0
constexpr int AUX = FUSED ? 16 : 0;
#define SC1 " sc1"

#define STAMP(kid, slot) do {} while (0)
#define STAMPC(kid, slot) do {} while (0)
#define TS(i) do {} while (0)
#define TS_DECL do {} while (0)
#define TS_FLUSH(kid) do {} while (0)
#define MFMA32(a, b, c) __builtin_amdgcn_mfma_f32_32x32x16_bf16(a, b, c, 0, 0, 0)
#define BAR() asm volatile("s_barrier" ::: "memory")
#define WAITV(n) asm volatile("s_waitcnt vmcnt(" #n ")" ::: "memory")
#define WAITL0() asm volatile("s_waitcnt lgkmcnt(0)" ::: "memory")
#define SB() __builtin_amdgcn_sched_barrier(0)

__device__ __forceinline__ unsigned cvtpk(float lo, float hi) {
    typedef __bf16 bf2 __attribute__((ext_vector_type(2)));
    bf2 r; r[0] = (__bf16)lo; r[1] = (__bf16)hi; return __builtin_bit_cast(unsigned, r);
}
__device__ __forceinline__ float bflo(unsigned u) { return __uint_as_float(u << 16); }
__device__ __forceinline__ float bfhi(unsigned u) { return __uint_as_float(u & 0xffff0000u); }
__device__ __forceinline__ float rbf(float f) { return __uint_as_float(cvtpk(f, 0.f) << 16); }
__device__ __forceinline__ s16x4 trread(LAS const char* p) {
    typedef short v4i16_t __attribute__((ext_vector_type(4)));
    return __builtin_amdgcn_ds_read_tr16_b64_v4i16((LAS v4i16_t*)p);
}
__device__ __forceinline__ bf16x8 cat8(s16x4 a, s16x4 b) {
    typedef short s16x8 __attribute__((ext_vector_type(8)));
    s16x8 r = __builtin_shufflevector(a, b, 0, 1, 2, 3, 4, 5, 6, 7);
    return __builtin_bit_cast(bf16x8, r);
}
__device__ __forceinline__ f32x16 smf(bf16x8 a, f32x8 b, f32x16 c) {
    return __builtin_amdgcn_smfmac_f32_32x32x32_bf16(a, __builtin_bit_cast(bf16x16, b), c, 0xDDDD, 0, 0);
}
__device__ __forceinline__ f32x8 lo8(f32x16 x) { return __builtin_shufflevector(x, x, 0, 1, 2, 3, 4, 5, 6, 7); }
__device__ __forceinline__ f32x8 hi8(f32x16 x) { return __builtin_shufflevector(x, x, 8, 9, 10, 11, 12, 13, 14, 15); }
__device__ __forceinline__ float swap32(float x) {
    auto r = __builtin_amdgcn_permlane32_swap(__float_as_uint(x), __float_as_uint(x), false, false);
    return __uint_as_float((threadIdx.x & 32) ? r[0] : r[1]);
}
template <int CTRL, int RM> __device__ __forceinline__ float dppmax(float v) {
    return __builtin_fmaxf(v, __builtin_bit_cast(float, __builtin_amdgcn_update_dpp(0, __builtin_bit_cast(int, v), CTRL, RM, 0xf, true)));
}
__device__ __forceinline__ float rowmax16(float v) { v = dppmax<0x111, 0xf>(v); v = dppmax<0x112, 0xf>(v); v = dppmax<0x114, 0xf>(v); return dppmax<0x118, 0xf>(v); }
__device__ __forceinline__ void dma16(srd_t srd, unsigned voff, unsigned soff, unsigned ldsaddr) {
    asm volatile("s_mov_b32 m0, %0\n\ts_nop 0\n\tbuffer_load_dwordx4 %1, %2, %3 offen lds" :: "s"(ldsaddr), "v"(voff), "s"(srd), "s"(soff) : "memory");
}
__device__ __forceinline__ void dma16c(srd_t srd, unsigned voff, unsigned soff, unsigned ldsaddr) {
    asm volatile("s_mov_b32 m0, %0\n\ts_nop 0\n\tbuffer_load_dwordx4 %1, %2, %3 offen" SC1 " lds" :: "s"(ldsaddr), "v"(voff), "s"(srd), "s"(soff) : "memory");
}
__device__ __forceinline__ void st16(srd_t srd, unsigned off, u32x4 v) { __builtin_amdgcn_raw_buffer_store_b128(v, srd, off, 0, AUX); }
__device__ __forceinline__ void st8(srd_t srd, unsigned off, u32x2 v) { __builtin_amdgcn_raw_buffer_store_b64(v, srd, off, 0, AUX); }
__device__ __forceinline__ void st4(srd_t srd, unsigned off, float v) { __builtin_amdgcn_raw_buffer_store_b32(__float_as_uint(v), srd, off, 0, AUX); }
__device__ __forceinline__ u32x4 ld16(srd_t srd, unsigned off) { return __builtin_amdgcn_raw_buffer_load_b128(srd, off, 0, AUX); }
typedef GAS unsigned long long gu64;
__device__ __forceinline__ void wait_flags(char* ws, unsigned off, int n, unsigned long long tag) {
    const int lane = threadIdx.x & 63;
    gu64* w = (gu64*)(ws + off) + (lane < n ? lane : 0);
    for (unsigned spins = 0; spins < (1u << 20); ++spins) {
        const unsigned long long v = __hip_atomic_load(w, __ATOMIC_RELAXED, __HIP_MEMORY_SCOPE_AGENT);
        if (__all(v == tag)) break;
        __builtin_amdgcn_s_sleep(1);
    }
    asm volatile("" ::: "memory");
}
__device__ __forceinline__ void put_flag(char* ws, unsigned off, unsigned long long tag) { __hip_atomic_store((gu64*)(ws + off), tag, __ATOMIC_RELAXED, __HIP_MEMORY_SCOPE_AGENT); }
__device__ __forceinline__ srd_t mksrd(const void* p, unsigned bytes) { return __builtin_amdgcn_make_buffer_rsrc((void*)p, (short)0, (int)bytes, 0x00020000); }

struct Params {
    const u16 *q, *k, *v, *shk, *ssk, *ssd, *x, *wqkv, *wo;
    u16* out; char* ws;
};

constexpr int P1_STAGE = 32768, P1_NST = 4, T1_LDS = 131072, NRM_LDS = 162912, FAST_LDS = NRM_LDS + 64;

__device__ __forceinline__ void phase1(const Params& p, LAS char* lds, unsigned long long tag) {
    char* ws = p.ws;
    const int tid = threadIdx.x, lane = tid & 63;
    const int wid = __builtin_amdgcn_readfirstlane(tid >> 6);
    const int bid = blockIdx.x, xc = bid & 7, jj = bid >> 3;
    const int rb = (xc & 3) * 8 + (jj & 7), g4 = (xc >> 2) * 4 + (jj >> 3);
    const int rot = (jj + 2 * xc) & 7;
    const srd_t sws = mksrd(ws, WS_BYTES);
    STAMP(0, 0);
    if (wid >= 8) {
        const int pw = wid - 8;
        const int slab = bid >> 3, cg = bid & 7;
        const int sl = pw * 16 + (lane >> 5) * 8, s0 = slab * 64 + sl, cl = (lane & 31) * 2, ch = cg * 64 + cl;
        BAR(); BAR(); BAR();
        unsigned kk[11], vv[8];
        const srd_t sk = mksrd(p.k, S * D * 2), sv = mksrd(p.v, S * D * 2);
        const int ko = ((s0 - 3) * D + ch) * 2;
#pragma unroll
        for (int i = 0; i < 11; ++i) kk[i] = __builtin_amdgcn_raw_buffer_load_b32(sk, (s0 - 3 + i >= 0) ? ko + i * D * 2 : 0x7ffffff0, 0, 0);
#pragma unroll
        for (int i = 0; i < 8; ++i) vv[i] = __builtin_amdgcn_raw_buffer_load_b32(sv, ((s0 + i) * D + ch) * 2, 0, 0);
        const u32x4 sw = *(const u32x4*)(p.shk + ch * 4);
        BAR(); BAR(); BAR(); BAR(); BAR();
        STAMP(0, 2);
        float w0[4], w1[4];
        w0[0] = bflo(sw[0]); w0[1] = bfhi(sw[0]); w0[2] = bflo(sw[1]); w0[3] = bfhi(sw[1]);
        w1[0] = bflo(sw[2]); w1[1] = bfhi(sw[2]); w1[2] = bflo(sw[3]); w1[3] = bfhi(sw[3]);
        float r0[8], r1[8];
#pragma unroll
        for (int i = 0; i < 8; ++i) {
            float a0 = bflo(kk[i]) * w0[3], a1 = bfhi(kk[i]) * w1[3];
            a0 += bflo(kk[i + 1]) * w0[2]; a1 += bfhi(kk[i + 1]) * w1[2];
            a0 += bflo(kk[i + 2]) * w0[1]; a1 += bfhi(kk[i + 2]) * w1[1];
            a0 += bflo(kk[i + 3]) * w0[0]; a1 += bfhi(kk[i + 3]) * w1[0];
            r0[i] = rbf(a0) * bflo(vv[i]); r1[i] = rbf(a1) * bfhi(vv[i]);
        }
        LAS char* tb = lds + T1_LDS;
        *(LAS u32x4*)(tb + cl * 144 + (7 - (sl >> 3)) * 16) = u32x4{cvtpk(r0[7], r0[6]), cvtpk(r0[5], r0[4]), cvtpk(r0[3], r0[2]), cvtpk(r0[1], r0[0])};
        *(LAS u32x4*)(tb + (cl + 1) * 144 + (7 - (sl >> 3)) * 16) = u32x4{cvtpk(r1[7], r1[6]), cvtpk(r1[5], r1[4]), cvtpk(r1[3], r1[2]), cvtpk(r1[1], r1[0])};
        WAITL0(); BAR();
#pragma unroll
        for (int i = 0; i < 2; ++i) { const int row = pw * 16 + i * 8 + (lane >> 3);
            st16(sws, WS_UR + ((cg * 64 + row) * S + (S - 64 - slab * 64)) * 2 + (lane & 7) * 16, *(LAS const u32x4*)(tb + row * 144 + (lane & 7) * 16)); }
        STAMP(0, 3);
        if (FUSED) WAITV(0);
        return;
    }
    if (wid >= 4) {
        const int pw = wid - 4;
        const srd_t sx = mksrd(p.x, S * D * 2), sw = mksrd(p.wqkv, D * 3 * D * 2);
        unsigned xo[2], wv[2];
#pragma unroll
        for (int i = 0; i < 2; ++i) { const int row = 8 * (2 * pw + i) + (lane >> 3), c = (lane & 7) ^ ((row >> 1) & 7); xo[i] = ((rb * 64 + row) * D + c * 8) * 2;
            wv[i] = row * 3072 + ((((lane & 7) >> 2) ^ ((lane >> 4) & 1)) * 64) + (lane & 3) * 16 + g4 * 128; }
        const unsigned l0 = (unsigned)(uintptr_t)lds + pw * 2048;
        auto issue = [&](int c) {
            const unsigned st = l0 + (c % P1_NST) * P1_STAGE; const int cc = (c + rot) & 7;
            const unsigned sox = cc * 128, sow = cc * 196608;
            asm volatile("s_mov_b32 m0, %[st]\n\ts_nop 0\n\tbuffer_load_dwordx4 %[x0], %[sx], %[sox] offen lds\n\t"
                         "s_add_u32 m0, %[st], 1024\n\ts_nop 0\n\tbuffer_load_dwordx4 %[x1], %[sx], %[sox] offen lds\n\t"
                         "s_add_u32 m0, %[st], 8192\n\ts_nop 0\n\tbuffer_load_dwordx4 %[w0], %[sw], %[sow] offen lds\n\t"
                         "s_add_u32 m0, %[st], 9216\n\ts_nop 0\n\tbuffer_load_dwordx4 %[w1], %[sw], %[sow] offen lds\n\t"
                         "s_add_u32 m0, %[st], 15360\n\ts_nop 0\n\tbuffer_load_dwordx4 %[w0], %[sw], %[sow] offen offset:1024 lds\n\t"
                         "s_add_u32 m0, %[st], 16384\n\ts_nop 0\n\tbuffer_load_dwordx4 %[w1], %[sw], %[sow] offen offset:1024 lds\n\t"
                         "s_add_u32 m0, %[st], 22528\n\ts_nop 0\n\tbuffer_load_dwordx4 %[w0], %[sw], %[sow] offen offset:2048 lds\n\t"
                         "s_add_u32 m0, %[st], 23552\n\ts_nop 0\n\tbuffer_load_dwordx4 %[w1], %[sw], %[sow] offen offset:2048 lds"
                         :: [st] "s"(st), [x0] "v"(xo[0]), [x1] "v"(xo[1]), [w0] "v"(wv[0]), [w1] "v"(wv[1]), [sx] "s"(sx), [sw] "s"(sw), [sox] "s"(sox), [sow] "s"(sow) : "scc", "memory");
        };
        TS_DECL; TS(0);
        issue(0); issue(1); TS(1);
        WAITV(8); TS(2); BAR(); issue(2); issue(3); TS(3);
        WAITV(16); TS(4); BAR(); issue(4); TS(5);
        WAITV(16); TS(6); BAR(); issue(5); TS(7);
        WAITV(16); TS(8); BAR(); issue(6); TS(9);
        WAITV(16); TS(10); BAR(); issue(7); TS(11);
        WAITV(16); TS(12); BAR();
        WAITV(8); TS(13); BAR();
        WAITV(0); TS(14); BAR();
        TS_FLUSH(3);
        BAR();
        if (wid == 4 && lane < 8) {
            const int hl = lane >> 1, kind = lane & 1;
            LAS const float* nr = (LAS const float*)(lds + NRM_LDS);
            const float v = __builtin_fmaxf(nr[((hl >> 1) * 2 + 0) * 4 + kind * 2 + (hl & 1)], nr[((hl >> 1) * 2 + 1) * 4 + kind * 2 + (hl & 1)]);
            put_flag(ws, WS_F1A + (((g4 * 4 + hl) * 32 + rb) * 2 + kind) * 8, (tag << 32) | __float_as_uint(v));
        }
        return;
    }
    const int i32 = lane & 31, hh = lane >> 5;
    const int tl = wid & 1, cf = wid >> 1;
    const int row = tl * 32 + i32, swz = (row >> 1) & 7;
    int xoff[4];
#pragma unroll
    for (int s = 0; s < 4; ++s) xoff[s] = row * 128 + (((2 * s + hh) ^ swz) * 16);
    const int qrow = (lane & 15) >> 2;
    const int woff = 8192 + (8 * hh + qrow) * 128 + ((cf ^ (qrow >> 1)) * 64) + ((lane >> 4) & 1) * 32 + (lane & 3) * 8;
    f32x16 aq = {}, ak = {}, av = {};
    bf16x8 xa[2], qa[2], ka[2], va[2], xb[2], qb[2], kb[2], vb[2];
#define P1_RD(X, Q, K, V, st, s0) do { _Pragma("unroll") for (int s_ = 0; s_ < 2; ++s_) { \
        LAS const char* wp_ = (st) + woff + ((s0) + s_) * 2048; \
        X[s_] = *(LAS const bf16x8*)((st) + xoff[(s0) + s_]); \
        Q[s_] = cat8(trread(wp_), trread(wp_ + 512)); K[s_] = cat8(trread(wp_ + 8192), trread(wp_ + 8192 + 512)); V[s_] = cat8(trread(wp_ + 16384), trread(wp_ + 16384 + 512)); } } while (0)
#define P1_MM(X, Q, K, V) do { _Pragma("unroll") for (int s_ = 0; s_ < 2; ++s_) { aq = MFMA32(Q[s_], X[s_], aq); ak = MFMA32(K[s_], X[s_], ak); av = MFMA32(X[s_], V[s_], av); } } while (0)
#define P1_MIX() do { _Pragma("unroll") for (int i_ = 0; i_ < 4; ++i_) { __builtin_amdgcn_sched_group_barrier(0x008, 1, 0); __builtin_amdgcn_sched_group_barrier(0x100, 3, 0); } \
        __builtin_amdgcn_sched_group_barrier(0x008, 1, 0); __builtin_amdgcn_sched_group_barrier(0x100, 2, 0); __builtin_amdgcn_sched_group_barrier(0x008, 1, 0); } while (0)
    TS_DECL;
#pragma unroll
    for (int c = 0; c < 8; ++c) {
        BAR();
        TS(c);
        LAS const char* st = lds + (c % P1_NST) * P1_STAGE;
        P1_RD(xa, qa, ka, va, st, 0);
        if (c > 0) { P1_MM(xb, qb, kb, vb); P1_MIX(); }
        SB();
        P1_RD(xb, qb, kb, vb, st, 2);
        P1_MM(xa, qa, ka, va); P1_MIX();
        SB();
        WAITL0();
    }
    TS(8);
    P1_MM(xb, qb, kb, vb);
    TS_FLUSH(3);
    STAMP(0, 9);
    const int tt = rb * 2 + tl;
    const int h0 = g4 * 4 + cf * 2;
    {
        const int e = i32 >> 4, d = i32 & 15;
#pragma unroll
        for (int sub = 0; sub < 2; ++sub)
            st16(sws, WS_VF + (((h0 + e) * 64 + tt) * 2 + sub) * 512 + hh * 256 + d * 16,
                 u32x4{cvtpk(av[8 * sub + 0], av[8 * sub + 1]), cvtpk(av[8 * sub + 2], av[8 * sub + 3]), cvtpk(av[8 * sub + 4], av[8 * sub + 5]), cvtpk(av[8 * sub + 6], av[8 * sub + 7])});
    }
    {
        const int pi = (i32 & 19) | ((i32 & 4) << 1) | ((i32 & 8) >> 1);
#pragma unroll
        for (int e = 0; e < 2; ++e) {
            float q8[8], k8[8]; float sq = 0.f, sk = 0.f;
#pragma unroll
            for (int j = 0; j < 8; ++j) { q8[j] = aq[8 * e + j] * QSCALE; k8[j] = ak[8 * e + j]; sq += q8[j] * q8[j]; sk += k8[j] * k8[j]; }
            st16(sws, WS_KF + (((h0 + e) * 64 + tt) * 64 + pi + 32 * hh) * 16, u32x4{cvtpk(k8[0], k8[1]), cvtpk(k8[2], k8[3]), cvtpk(k8[4], k8[5]), cvtpk(k8[6], k8[7])});
            st16(sws, WS_QF + (((h0 + e) * 64 + tt) * 64 + i32 + 32 * hh) * 16, u32x4{cvtpk(q8[0], q8[1]), cvtpk(q8[2], q8[3]), cvtpk(q8[4], q8[5]), cvtpk(q8[6], q8[7])});
            sq += swap32(sq); sk += swap32(sk);
            sq = dppmax<0x142, 0xa>(rowmax16(sq)); sk = dppmax<0x142, 0xa>(rowmax16(sk));
            if (lane == 31) { LAS float* nr = (LAS float*)(lds + NRM_LDS) + (cf * 2 + tl) * 4; nr[e] = sk; nr[2 + e] = sq; }
        }
    }
    STAMP(0, 10);
    if (FUSED) WAITV(0);
    WAITL0(); BAR();
}

#define ATTN_LOOP_ASM \
  "s_waitcnt vmcnt(%[w0])\n" \
  "s_barrier\n" \
  "v_mov_b32 v116, 0xdddd\n" \
  "ds_read_b128 v[96:99], %[ka] offset:0\n" \
  "v_mov_b32 v100, %[fill]\n" \
  "v_mov_b32 v101, %[fill]\n" \
  "v_mov_b32 v102, %[fill]\n" \
  "v_mov_b32 v103, %[fill]\n" \
  "v_mov_b32 v104, %[fill]\n" \
  "v_mov_b32 v105, %[fill]\n" \
  "v_mov_b32 v106, %[fill]\n" \
  "v_mov_b32 v107, %[fill]\n" \
  "v_mov_b32 v108, %[fill]\n" \
  "v_mov_b32 v109, %[fill]\n" \
  "v_mov_b32 v110, %[fill]\n" \
  "v_mov_b32 v111, %[fill]\n" \
  "v_mov_b32 v112, %[fill]\n" \
  "v_mov_b32 v113, %[fill]\n" \
  "v_mov_b32 v114, %[fill]\n" \
  "v_mov_b32 v115, %[fill]\n" \
  "s_mov_b64 exec, %[vm]\n" \
  "ds_read_b128 v[100:103], %[va] offset:0\n" \
  "ds_read_b128 v[104:107], %[va] offset:512\n" \
  "s_mov_b64 exec, -1\n" \
  "s_waitcnt lgkmcnt(2)\n" \
  "v_mfma_f32_32x32x16_bf16 v[64:79], v[96:99], %[q1], 0\n" \
  "v_mfma_f32_32x32x16_bf16 v[80:95], v[96:99], %[q2], 0\n" \
  "s_nop 7\n" \
  "s_nop 3\n" \
  "v_exp_f32 v64, v64\n" \
  "v_exp_f32 v65, v65\n" \
  "v_exp_f32 v66, v66\n" \
  "v_exp_f32 v67, v67\n" \
  "v_exp_f32 v68, v68\n" \
  "s_mov_b32 %[g], 0\n" \
  "LOOP%=:\n" \
  "s_cmp_eq_u32 %[g], 1\n" \
  "s_cbranch_scc1 W1%=\n" \
  "s_cmp_eq_u32 %[g], 3\n" \
  "s_cbranch_scc1 W3%=\n" \
  "BODY%=:\n" \
  "s_mov_b64 exec, %[vm]\n" \
  "ds_read_b128 v[108:111], %[va] offset:1024\n" \
  "ds_read_b128 v[112:115], %[va] offset:1536\n" \
  "s_mov_b64 exec, -1\n" \
  "ds_read_b128 v[96:99], %[ka] offset:1024\n" \
  "v_exp_f32 v69, v69\n" \
  "v_exp_f32 v70, v70\n" \
  "v_exp_f32 v71, v71\n" \
  "v_exp_f32 v72, v72\n" \
  "v_exp_f32 v73, v73\n" \
  "s_waitcnt lgkmcnt(3)\n" \
  "v_smfmac_f32_32x32x32_bf16 %[o1], v[100:103], v[64:71], v116\n" \
  "v_exp_f32 v74, v74\n" \
  "v_exp_f32 v75, v75\n" \
  "v_exp_f32 v76, v76\n" \
  "v_exp_f32 v77, v77\n" \
  "v_exp_f32 v78, v78\n" \
  "v_exp_f32 v79, v79\n" \
  "v_exp_f32 v80, v80\n" \
  "v_exp_f32 v81, v81\n" \
  "v_smfmac_f32_32x32x32_bf16 %[o1], v[104:107], v[72:79], v116\n" \
  "v_exp_f32 v82, v82\n" \
  "v_exp_f32 v83, v83\n" \
  "v_exp_f32 v84, v84\n" \
  "s_waitcnt lgkmcnt(0)\n" \
  "v_mfma_f32_32x32x16_bf16 v[64:79], v[96:99], %[q1], 0\n" \
  "v_exp_f32 v85, v85\n" \
  "v_exp_f32 v86, v86\n" \
  "v_exp_f32 v87, v87\n" \
  "v_exp_f32 v88, v88\n" \
  "v_exp_f32 v89, v89\n" \
  "v_smfmac_f32_32x32x32_bf16 %[o2], v[100:103], v[80:87], v116\n" \
  "v_exp_f32 v90, v90\n" \
  "v_exp_f32 v91, v91\n" \
  "v_exp_f32 v92, v92\n" \
  "v_exp_f32 v93, v93\n" \
  "v_exp_f32 v94, v94\n" \
  "v_exp_f32 v95, v95\n" \
  "v_exp_f32 v64, v64\n" \
  "v_exp_f32 v65, v65\n" \
  "v_smfmac_f32_32x32x32_bf16 %[o2], v[104:107], v[88:95], v116\n" \
  "v_exp_f32 v66, v66\n" \
  "v_exp_f32 v67, v67\n" \
  "v_exp_f32 v68, v68\n" \
  "v_mfma_f32_32x32x16_bf16 v[80:95], v[96:99], %[q2], 0\n" \
  "s_mov_b64 exec, %[vm]\n" \
  "ds_read_b128 v[100:103], %[va] offset:2048\n" \
  "ds_read_b128 v[104:107], %[va] offset:2560\n" \
  "s_mov_b64 exec, -1\n" \
  "ds_read_b128 v[96:99], %[ka] offset:2048\n" \
  "v_exp_f32 v69, v69\n" \
  "v_exp_f32 v70, v70\n" \
  "v_exp_f32 v71, v71\n" \
  "v_exp_f32 v72, v72\n" \
  "v_exp_f32 v73, v73\n" \
  "s_waitcnt lgkmcnt(3)\n" \
  "v_smfmac_f32_32x32x32_bf16 %[o1], v[108:111], v[64:71], v116\n" \
  "v_exp_f32 v74, v74\n" \
  "v_exp_f32 v75, v75\n" \
  "v_exp_f32 v76, v76\n" \
  "v_exp_f32 v77, v77\n" \
  "v_exp_f32 v78, v78\n" \
  "v_exp_f32 v79, v79\n" \
  "v_exp_f32 v80, v80\n" \
  "v_exp_f32 v81, v81\n" \
  "v_smfmac_f32_32x32x32_bf16 %[o1], v[112:115], v[72:79], v116\n" \
  "v_exp_f32 v82, v82\n" \
  "v_exp_f32 v83, v83\n" \
  "v_exp_f32 v84, v84\n" \
  "s_waitcnt lgkmcnt(0)\n" \
  "v_mfma_f32_32x32x16_bf16 v[64:79], v[96:99], %[q1], 0\n" \
  "v_exp_f32 v85, v85\n" \
  "v_exp_f32 v86, v86\n" \
  "v_exp_f32 v87, v87\n" \
  "v_exp_f32 v88, v88\n" \
  "v_exp_f32 v89, v89\n" \
  "v_smfmac_f32_32x32x32_bf16 %[o2], v[108:111], v[80:87], v116\n" \
  "v_exp_f32 v90, v90\n" \
  "v_exp_f32 v91, v91\n" \
  "v_exp_f32 v92, v92\n" \
  "v_exp_f32 v93, v93\n" \
  "v_exp_f32 v94, v94\n" \
  "v_exp_f32 v95, v95\n" \
  "v_exp_f32 v64, v64\n" \
  "v_exp_f32 v65, v65\n" \
  "v_smfmac_f32_32x32x32_bf16 %[o2], v[112:115], v[88:95], v116\n" \
  "v_exp_f32 v66, v66\n" \
  "v_exp_f32 v67, v67\n" \
  "v_exp_f32 v68, v68\n" \
  "v_mfma_f32_32x32x16_bf16 v[80:95], v[96:99], %[q2], 0\n" \
  "s_mov_b64 exec, %[vm]\n" \
  "ds_read_b128 v[108:111], %[va] offset:3072\n" \
  "ds_read_b128 v[112:115], %[va] offset:3584\n" \
  "s_mov_b64 exec, -1\n" \
  "ds_read_b128 v[96:99], %[ka] offset:3072\n" \
  "v_exp_f32 v69, v69\n" \
  "v_exp_f32 v70, v70\n" \
  "v_exp_f32 v71, v71\n" \
  "v_exp_f32 v72, v72\n" \
  "v_exp_f32 v73, v73\n" \
  "s_waitcnt lgkmcnt(3)\n" \
  "v_smfmac_f32_32x32x32_bf16 %[o1], v[100:103], v[64:71], v116\n" \
  "v_exp_f32 v74, v74\n" \
  "v_exp_f32 v75, v75\n" \
  "v_exp_f32 v76, v76\n" \
  "v_exp_f32 v77, v77\n" \
  "v_exp_f32 v78, v78\n" \
  "v_exp_f32 v79, v79\n" \
  "v_exp_f32 v80, v80\n" \
  "v_exp_f32 v81, v81\n" \
  "v_smfmac_f32_32x32x32_bf16 %[o1], v[104:107], v[72:79], v116\n" \
  "v_exp_f32 v82, v82\n" \
  "v_exp_f32 v83, v83\n" \
  "v_exp_f32 v84, v84\n" \
  "s_waitcnt lgkmcnt(0)\n" \
  "v_mfma_f32_32x32x16_bf16 v[64:79], v[96:99], %[q1], 0\n" \
  "v_exp_f32 v85, v85\n" \
  "v_exp_f32 v86, v86\n" \
  "v_exp_f32 v87, v87\n" \
  "v_exp_f32 v88, v88\n" \
  "v_exp_f32 v89, v89\n" \
  "v_smfmac_f32_32x32x32_bf16 %[o2], v[100:103], v[80:87], v116\n" \
  "v_exp_f32 v90, v90\n" \
  "v_exp_f32 v91, v91\n" \
  "v_exp_f32 v92, v92\n" \
  "v_exp_f32 v93, v93\n" \
  "v_exp_f32 v94, v94\n" \
  "v_exp_f32 v95, v95\n" \
  "v_exp_f32 v64, v64\n" \
  "v_exp_f32 v65, v65\n" \
  "v_smfmac_f32_32x32x32_bf16 %[o2], v[104:107], v[88:95], v116\n" \
  "v_exp_f32 v66, v66\n" \
  "v_exp_f32 v67, v67\n" \
  "v_exp_f32 v68, v68\n" \
  "v_mfma_f32_32x32x16_bf16 v[80:95], v[96:99], %[q2], 0\n" \
  "s_mov_b64 exec, %[vm]\n" \
  "ds_read_b128 v[100:103], %[va] offset:4096\n" \
  "ds_read_b128 v[104:107], %[va] offset:4608\n" \
  "s_mov_b64 exec, -1\n" \
  "ds_read_b128 v[96:99], %[ka] offset:4096\n" \
  "v_exp_f32 v69, v69\n" \
  "v_exp_f32 v70, v70\n" \
  "v_exp_f32 v71, v71\n" \
  "v_exp_f32 v72, v72\n" \
  "v_exp_f32 v73, v73\n" \
  "s_waitcnt lgkmcnt(3)\n" \
  "v_smfmac_f32_32x32x32_bf16 %[o1], v[108:111], v[64:71], v116\n" \
  "v_exp_f32 v74, v74\n" \
  "v_exp_f32 v75, v75\n" \
  "v_exp_f32 v76, v76\n" \
  "v_exp_f32 v77, v77\n" \
  "v_exp_f32 v78, v78\n" \
  "v_exp_f32 v79, v79\n" \
  "v_exp_f32 v80, v80\n" \
  "v_exp_f32 v81, v81\n" \
  "v_smfmac_f32_32x32x32_bf16 %[o1], v[112:115], v[72:79], v116\n" \
  "v_exp_f32 v82, v82\n" \
  "v_exp_f32 v83, v83\n" \
  "v_exp_f32 v84, v84\n" \
  "s_waitcnt lgkmcnt(0)\n" \
  "v_mfma_f32_32x32x16_bf16 v[64:79], v[96:99], %[q1], 0\n" \
  "v_exp_f32 v85, v85\n" \
  "v_exp_f32 v86, v86\n" \
  "v_exp_f32 v87, v87\n" \
  "v_exp_f32 v88, v88\n" \
  "v_exp_f32 v89, v89\n" \
  "v_smfmac_f32_32x32x32_bf16 %[o2], v[108:111], v[80:87], v116\n" \
  "v_exp_f32 v90, v90\n" \
  "v_exp_f32 v91, v91\n" \
  "v_exp_f32 v92, v92\n" \
  "v_exp_f32 v93, v93\n" \
  "v_exp_f32 v94, v94\n" \
  "v_exp_f32 v95, v95\n" \
  "v_exp_f32 v64, v64\n" \
  "v_exp_f32 v65, v65\n" \
  "v_smfmac_f32_32x32x32_bf16 %[o2], v[112:115], v[88:95], v116\n" \
  "v_exp_f32 v66, v66\n" \
  "v_exp_f32 v67, v67\n" \
  "v_exp_f32 v68, v68\n" \
  "v_mfma_f32_32x32x16_bf16 v[80:95], v[96:99], %[q2], 0\n" \
  "v_add_u32 %[ka], 4096, %[ka]\n" \
  "v_add_u32 %[va], 4096, %[va]\n" \
  "s_add_u32 %[g], %[g], 1\n" \
  "s_cmp_lt_u32 %[g], 8\n" \
  "s_cbranch_scc1 LOOP%=\n" \
  "s_branch END%=\n" \
  "W1%=:\n" \
  "s_waitcnt vmcnt(%[w1])\n" \
  "s_barrier\n" \
  "s_branch BODY%=\n" \
  "W3%=:\n" \
  "s_waitcnt vmcnt(0)\n" \
  "s_barrier\n" \
  "s_branch BODY%=\n" \
  "END%=:\n" \
  "s_waitcnt lgkmcnt(0)\n" \
  "s_nop 15\n" \
  "s_nop 7\n"
#define ATTN_LOOP_CLOBBERS "v64", "v65", "v66", "v67", "v68", "v69", "v70", "v71", "v72", "v73", "v74", "v75", "v76", "v77", "v78", "v79", "v80", "v81", "v82", "v83", "v84", "v85", "v86", "v87", "v88", "v89", "v90", "v91", "v92", "v93", "v94", "v95", "v96", "v97", "v98", "v99", "v100", "v101", "v102", "v103", "v104", "v105", "v106", "v107", "v108", "v109", "v110", "v111", "v112", "v113", "v114", "v115", "v116"
#define CONV_LOOP_ASM \
  "ds_read2_b32 v[64:65], %[ap] offset0:0 offset1:1\n" \
  "ds_read2_b32 v[66:67], %[ap] offset0:2 offset1:3\n" \
  "ds_read_b128 v[76:79], %[bp] offset:0\n" \
  "ds_read_b128 v[72:75], %[bp] offset:2560\n" \
  "ds_read2_b32 v[68:69], %[aq] offset0:0 offset1:1\n" \
  "ds_read2_b32 v[70:71], %[aq] offset0:2 offset1:3\n" \
  "ds_read2_b32 v[80:81], %[ap] offset0:8 offset1:9\n" \
  "ds_read2_b32 v[82:83], %[ap] offset0:10 offset1:11\n" \
  "s_waitcnt lgkmcnt(2)\n" \
  "v_mfma_f32_32x32x16_bf16 %[a1], v[64:67], v[76:79], %[a1]\n" \
  "ds_read_b128 v[92:95], %[bp] offset:32\n" \
  "ds_read_b128 v[88:91], %[bp] offset:2592\n" \
  "v_mfma_f32_32x32x16_bf16 %[a0], v[64:67], v[72:75], %[a0]\n" \
  "ds_read2_b32 v[84:85], %[aq] offset0:8 offset1:9\n" \
  "ds_read2_b32 v[86:87], %[aq] offset0:10 offset1:11\n" \
  "v_mfma_f32_32x32x16_bf16 %[a1], v[68:71], v[72:75], %[a1]\n" \
  "ds_read2_b32 v[64:65], %[ap] offset0:16 offset1:17\n" \
  "ds_read2_b32 v[66:67], %[ap] offset0:18 offset1:19\n" \
  "s_waitcnt lgkmcnt(2)\n" \
  "v_mfma_f32_32x32x16_bf16 %[a1], v[80:83], v[92:95], %[a1]\n" \
  "ds_read_b128 v[76:79], %[bp] offset:80\n" \
  "ds_read_b128 v[72:75], %[bp] offset:2640\n" \
  "v_mfma_f32_32x32x16_bf16 %[a0], v[80:83], v[88:91], %[a0]\n" \
  "ds_read2_b32 v[68:69], %[aq] offset0:16 offset1:17\n" \
  "ds_read2_b32 v[70:71], %[aq] offset0:18 offset1:19\n" \
  "v_mfma_f32_32x32x16_bf16 %[a1], v[84:87], v[88:91], %[a1]\n" \
  "ds_read2_b32 v[80:81], %[ap] offset0:24 offset1:25\n" \
  "ds_read2_b32 v[82:83], %[ap] offset0:26 offset1:27\n" \
  "s_waitcnt lgkmcnt(2)\n" \
  "v_mfma_f32_32x32x16_bf16 %[a1], v[64:67], v[76:79], %[a1]\n" \
  "ds_read_b128 v[92:95], %[bp] offset:112\n" \
  "ds_read_b128 v[88:91], %[bp] offset:2672\n" \
  "v_mfma_f32_32x32x16_bf16 %[a0], v[64:67], v[72:75], %[a0]\n" \
  "ds_read2_b32 v[84:85], %[aq] offset0:24 offset1:25\n" \
  "ds_read2_b32 v[86:87], %[aq] offset0:26 offset1:27\n" \
  "v_mfma_f32_32x32x16_bf16 %[a1], v[68:71], v[72:75], %[a1]\n" \
  "ds_read2_b32 v[64:65], %[ap] offset0:32 offset1:33\n" \
  "ds_read2_b32 v[66:67], %[ap] offset0:34 offset1:35\n" \
  "s_waitcnt lgkmcnt(2)\n" \
  "v_mfma_f32_32x32x16_bf16 %[a1], v[80:83], v[92:95], %[a1]\n" \
  "ds_read_b128 v[76:79], %[bp] offset:160\n" \
  "ds_read_b128 v[72:75], %[bp] offset:2720\n" \
  "v_mfma_f32_32x32x16_bf16 %[a0], v[80:83], v[88:91], %[a0]\n" \
  "ds_read2_b32 v[68:69], %[aq] offset0:32 offset1:33\n" \
  "ds_read2_b32 v[70:71], %[aq] offset0:34 offset1:35\n" \
  "v_mfma_f32_32x32x16_bf16 %[a1], v[84:87], v[88:91], %[a1]\n" \
  "ds_read2_b32 v[80:81], %[ap] offset0:40 offset1:41\n" \
  "ds_read2_b32 v[82:83], %[ap] offset0:42 offset1:43\n" \
  "s_waitcnt lgkmcnt(2)\n" \
  "v_mfma_f32_32x32x16_bf16 %[a1], v[64:67], v[76:79], %[a1]\n" \
  "ds_read_b128 v[92:95], %[bp] offset:192\n" \
  "ds_read_b128 v[88:91], %[bp] offset:2752\n" \
  "v_mfma_f32_32x32x16_bf16 %[a0], v[64:67], v[72:75], %[a0]\n" \
  "ds_read2_b32 v[84:85], %[aq] offset0:40 offset1:41\n" \
  "ds_read2_b32 v[86:87], %[aq] offset0:42 offset1:43\n" \
  "v_mfma_f32_32x32x16_bf16 %[a1], v[68:71], v[72:75], %[a1]\n" \
  "ds_read2_b32 v[64:65], %[ap] offset0:48 offset1:49\n" \
  "ds_read2_b32 v[66:67], %[ap] offset0:50 offset1:51\n" \
  "s_waitcnt lgkmcnt(2)\n" \
  "v_mfma_f32_32x32x16_bf16 %[a1], v[80:83], v[92:95], %[a1]\n" \
  "ds_read_b128 v[76:79], %[bp] offset:240\n" \
  "ds_read_b128 v[72:75], %[bp] offset:2800\n" \
  "v_mfma_f32_32x32x16_bf16 %[a0], v[80:83], v[88:91], %[a0]\n" \
  "ds_read2_b32 v[68:69], %[aq] offset0:48 offset1:49\n" \
  "ds_read2_b32 v[70:71], %[aq] offset0:50 offset1:51\n" \
  "v_mfma_f32_32x32x16_bf16 %[a1], v[84:87], v[88:91], %[a1]\n" \
  "ds_read2_b32 v[80:81], %[ap] offset0:56 offset1:57\n" \
  "ds_read2_b32 v[82:83], %[ap] offset0:58 offset1:59\n" \
  "s_waitcnt lgkmcnt(2)\n" \
  "v_mfma_f32_32x32x16_bf16 %[a1], v[64:67], v[76:79], %[a1]\n" \
  "ds_read_b128 v[92:95], %[bp] offset:272\n" \
  "ds_read_b128 v[88:91], %[bp] offset:2832\n" \
  "v_mfma_f32_32x32x16_bf16 %[a0], v[64:67], v[72:75], %[a0]\n" \
  "ds_read2_b32 v[84:85], %[aq] offset0:56 offset1:57\n" \
  "ds_read2_b32 v[86:87], %[aq] offset0:58 offset1:59\n" \
  "v_mfma_f32_32x32x16_bf16 %[a1], v[68:71], v[72:75], %[a1]\n" \
  "ds_read2_b32 v[64:65], %[ap] offset0:64 offset1:65\n" \
  "ds_read2_b32 v[66:67], %[ap] offset0:66 offset1:67\n" \
  "s_waitcnt lgkmcnt(2)\n" \
  "v_mfma_f32_32x32x16_bf16 %[a1], v[80:83], v[92:95], %[a1]\n" \
  "ds_read_b128 v[76:79], %[bp] offset:320\n" \
  "ds_read_b128 v[72:75], %[bp] offset:2880\n" \
  "v_mfma_f32_32x32x16_bf16 %[a0], v[80:83], v[88:91], %[a0]\n" \
  "ds_read2_b32 v[68:69], %[aq] offset0:64 offset1:65\n" \
  "ds_read2_b32 v[70:71], %[aq] offset0:66 offset1:67\n" \
  "v_mfma_f32_32x32x16_bf16 %[a1], v[84:87], v[88:91], %[a1]\n" \
  "ds_read2_b32 v[80:81], %[ap] offset0:72 offset1:73\n" \
  "ds_read2_b32 v[82:83], %[ap] offset0:74 offset1:75\n" \
  "s_waitcnt lgkmcnt(2)\n" \
  "v_mfma_f32_32x32x16_bf16 %[a1], v[64:67], v[76:79], %[a1]\n" \
  "ds_read_b128 v[92:95], %[bp] offset:352\n" \
  "ds_read_b128 v[88:91], %[bp] offset:2912\n" \
  "v_mfma_f32_32x32x16_bf16 %[a0], v[64:67], v[72:75], %[a0]\n" \
  "ds_read2_b32 v[84:85], %[aq] offset0:72 offset1:73\n" \
  "ds_read2_b32 v[86:87], %[aq] offset0:74 offset1:75\n" \
  "v_mfma_f32_32x32x16_bf16 %[a1], v[68:71], v[72:75], %[a1]\n" \
  "ds_read2_b32 v[64:65], %[ap] offset0:80 offset1:81\n" \
  "ds_read2_b32 v[66:67], %[ap] offset0:82 offset1:83\n" \
  "s_waitcnt lgkmcnt(2)\n" \
  "v_mfma_f32_32x32x16_bf16 %[a1], v[80:83], v[92:95], %[a1]\n" \
  "ds_read_b128 v[76:79], %[bp] offset:400\n" \
  "ds_read_b128 v[72:75], %[bp] offset:2960\n" \
  "v_mfma_f32_32x32x16_bf16 %[a0], v[80:83], v[88:91], %[a0]\n" \
  "ds_read2_b32 v[68:69], %[aq] offset0:80 offset1:81\n" \
  "ds_read2_b32 v[70:71], %[aq] offset0:82 offset1:83\n" \
  "v_mfma_f32_32x32x16_bf16 %[a1], v[84:87], v[88:91], %[a1]\n" \
  "ds_read2_b32 v[80:81], %[ap] offset0:88 offset1:89\n" \
  "ds_read2_b32 v[82:83], %[ap] offset0:90 offset1:91\n" \
  "s_waitcnt lgkmcnt(2)\n" \
  "v_mfma_f32_32x32x16_bf16 %[a1], v[64:67], v[76:79], %[a1]\n" \
  "ds_read_b128 v[92:95], %[bp] offset:432\n" \
  "ds_read_b128 v[88:91], %[bp] offset:2992\n" \
  "v_mfma_f32_32x32x16_bf16 %[a0], v[64:67], v[72:75], %[a0]\n" \
  "ds_read2_b32 v[84:85], %[aq] offset0:88 offset1:89\n" \
  "ds_read2_b32 v[86:87], %[aq] offset0:90 offset1:91\n" \
  "v_mfma_f32_32x32x16_bf16 %[a1], v[68:71], v[72:75], %[a1]\n" \
  "ds_read2_b32 v[64:65], %[ap] offset0:96 offset1:97\n" \
  "ds_read2_b32 v[66:67], %[ap] offset0:98 offset1:99\n" \
  "s_waitcnt lgkmcnt(2)\n" \
  "v_mfma_f32_32x32x16_bf16 %[a1], v[80:83], v[92:95], %[a1]\n" \
  "ds_read_b128 v[76:79], %[bp] offset:480\n" \
  "ds_read_b128 v[72:75], %[bp] offset:3040\n" \
  "v_mfma_f32_32x32x16_bf16 %[a0], v[80:83], v[88:91], %[a0]\n" \
  "ds_read2_b32 v[68:69], %[aq] offset0:96 offset1:97\n" \
  "ds_read2_b32 v[70:71], %[aq] offset0:98 offset1:99\n" \
  "v_mfma_f32_32x32x16_bf16 %[a1], v[84:87], v[88:91], %[a1]\n" \
  "ds_read2_b32 v[80:81], %[ap] offset0:104 offset1:105\n" \
  "ds_read2_b32 v[82:83], %[ap] offset0:106 offset1:107\n" \
  "s_waitcnt lgkmcnt(2)\n" \
  "v_mfma_f32_32x32x16_bf16 %[a1], v[64:67], v[76:79], %[a1]\n" \
  "ds_read_b128 v[92:95], %[bp] offset:512\n" \
  "ds_read_b128 v[88:91], %[bp] offset:3072\n" \
  "v_mfma_f32_32x32x16_bf16 %[a0], v[64:67], v[72:75], %[a0]\n" \
  "ds_read2_b32 v[84:85], %[aq] offset0:104 offset1:105\n" \
  "ds_read2_b32 v[86:87], %[aq] offset0:106 offset1:107\n" \
  "v_mfma_f32_32x32x16_bf16 %[a1], v[68:71], v[72:75], %[a1]\n" \
  "ds_read2_b32 v[64:65], %[ap] offset0:112 offset1:113\n" \
  "ds_read2_b32 v[66:67], %[ap] offset0:114 offset1:115\n" \
  "s_waitcnt lgkmcnt(2)\n" \
  "v_mfma_f32_32x32x16_bf16 %[a1], v[80:83], v[92:95], %[a1]\n" \
  "ds_read_b128 v[76:79], %[bp] offset:560\n" \
  "ds_read_b128 v[72:75], %[bp] offset:3120\n" \
  "v_mfma_f32_32x32x16_bf16 %[a0], v[80:83], v[88:91], %[a0]\n" \
  "ds_read2_b32 v[68:69], %[aq] offset0:112 offset1:113\n" \
  "ds_read2_b32 v[70:71], %[aq] offset0:114 offset1:115\n" \
  "v_mfma_f32_32x32x16_bf16 %[a1], v[84:87], v[88:91], %[a1]\n" \
  "ds_read2_b32 v[80:81], %[ap] offset0:120 offset1:121\n" \
  "ds_read2_b32 v[82:83], %[ap] offset0:122 offset1:123\n" \
  "s_waitcnt lgkmcnt(2)\n" \
  "v_mfma_f32_32x32x16_bf16 %[a1], v[64:67], v[76:79], %[a1]\n" \
  "ds_read_b128 v[92:95], %[bp] offset:592\n" \
  "ds_read_b128 v[88:91], %[bp] offset:3152\n" \
  "v_mfma_f32_32x32x16_bf16 %[a0], v[64:67], v[72:75], %[a0]\n" \
  "ds_read2_b32 v[84:85], %[aq] offset0:120 offset1:121\n" \
  "ds_read2_b32 v[86:87], %[aq] offset0:122 offset1:123\n" \
  "v_mfma_f32_32x32x16_bf16 %[a1], v[68:71], v[72:75], %[a1]\n" \
  "ds_read2_b32 v[64:65], %[ap] offset0:128 offset1:129\n" \
  "ds_read2_b32 v[66:67], %[ap] offset0:130 offset1:131\n" \
  "s_waitcnt lgkmcnt(2)\n" \
  "v_mfma_f32_32x32x16_bf16 %[a1], v[80:83], v[92:95], %[a1]\n" \
  "ds_read_b128 v[76:79], %[bp] offset:640\n" \
  "ds_read_b128 v[72:75], %[bp] offset:3200\n" \
  "v_mfma_f32_32x32x16_bf16 %[a0], v[80:83], v[88:91], %[a0]\n" \
  "ds_read2_b32 v[68:69], %[aq] offset0:128 offset1:129\n" \
  "ds_read2_b32 v[70:71], %[aq] offset0:130 offset1:131\n" \
  "v_mfma_f32_32x32x16_bf16 %[a1], v[84:87], v[88:91], %[a1]\n" \
  "ds_read2_b32 v[80:81], %[ap] offset0:136 offset1:137\n" \
  "ds_read2_b32 v[82:83], %[ap] offset0:138 offset1:139\n" \
  "s_waitcnt lgkmcnt(2)\n" \
  "v_mfma_f32_32x32x16_bf16 %[a1], v[64:67], v[76:79], %[a1]\n" \
  "ds_read_b128 v[92:95], %[bp] offset:672\n" \
  "ds_read_b128 v[88:91], %[bp] offset:3232\n" \
  "v_mfma_f32_32x32x16_bf16 %[a0], v[64:67], v[72:75], %[a0]\n" \
  "ds_read2_b32 v[84:85], %[aq] offset0:136 offset1:137\n" \
  "ds_read2_b32 v[86:87], %[aq] offset0:138 offset1:139\n" \
  "v_mfma_f32_32x32x16_bf16 %[a1], v[68:71], v[72:75], %[a1]\n" \
  "ds_read2_b32 v[64:65], %[ap] offset0:144 offset1:145\n" \
  "ds_read2_b32 v[66:67], %[ap] offset0:146 offset1:147\n" \
  "s_waitcnt lgkmcnt(2)\n" \
  "v_mfma_f32_32x32x16_bf16 %[a1], v[80:83], v[92:95], %[a1]\n" \
  "ds_read_b128 v[76:79], %[bp] offset:720\n" \
  "ds_read_b128 v[72:75], %[bp] offset:3280\n" \
  "v_mfma_f32_32x32x16_bf16 %[a0], v[80:83], v[88:91], %[a0]\n" \
  "ds_read2_b32 v[68:69], %[aq] offset0:144 offset1:145\n" \
  "ds_read2_b32 v[70:71], %[aq] offset0:146 offset1:147\n" \
  "v_mfma_f32_32x32x16_bf16 %[a1], v[84:87], v[88:91], %[a1]\n" \
  "ds_read2_b32 v[80:81], %[ap] offset0:152 offset1:153\n" \
  "ds_read2_b32 v[82:83], %[ap] offset0:154 offset1:155\n" \
  "s_waitcnt lgkmcnt(2)\n" \
  "v_mfma_f32_32x32x16_bf16 %[a1], v[64:67], v[76:79], %[a1]\n" \
  "ds_read_b128 v[92:95], %[bp] offset:752\n" \
  "ds_read_b128 v[88:91], %[bp] offset:3312\n" \
  "v_mfma_f32_32x32x16_bf16 %[a0], v[64:67], v[72:75], %[a0]\n" \
  "ds_read2_b32 v[84:85], %[aq] offset0:152 offset1:153\n" \
  "ds_read2_b32 v[86:87], %[aq] offset0:154 offset1:155\n" \
  "v_mfma_f32_32x32x16_bf16 %[a1], v[68:71], v[72:75], %[a1]\n" \
  "ds_read2_b32 v[64:65], %[ap] offset0:160 offset1:161\n" \
  "ds_read2_b32 v[66:67], %[ap] offset0:162 offset1:163\n" \
  "s_waitcnt lgkmcnt(2)\n" \
  "v_mfma_f32_32x32x16_bf16 %[a1], v[80:83], v[92:95], %[a1]\n" \
  "ds_read_b128 v[76:79], %[bp] offset:800\n" \
  "ds_read_b128 v[72:75], %[bp] offset:3360\n" \
  "v_mfma_f32_32x32x16_bf16 %[a0], v[80:83], v[88:91], %[a0]\n" \
  "ds_read2_b32 v[68:69], %[aq] offset0:160 offset1:161\n" \
  "ds_read2_b32 v[70:71], %[aq] offset0:162 offset1:163\n" \
  "v_mfma_f32_32x32x16_bf16 %[a1], v[84:87], v[88:91], %[a1]\n" \
  "ds_read2_b32 v[80:81], %[ap] offset0:168 offset1:169\n" \
  "ds_read2_b32 v[82:83], %[ap] offset0:170 offset1:171\n" \
  "s_waitcnt lgkmcnt(2)\n" \
  "v_mfma_f32_32x32x16_bf16 %[a1], v[64:67], v[76:79], %[a1]\n" \
  "ds_read_b128 v[92:95], %[bp] offset:832\n" \
  "ds_read_b128 v[88:91], %[bp] offset:3392\n" \
  "v_mfma_f32_32x32x16_bf16 %[a0], v[64:67], v[72:75], %[a0]\n" \
  "ds_read2_b32 v[84:85], %[aq] offset0:168 offset1:169\n" \
  "ds_read2_b32 v[86:87], %[aq] offset0:170 offset1:171\n" \
  "v_mfma_f32_32x32x16_bf16 %[a1], v[68:71], v[72:75], %[a1]\n" \
  "ds_read2_b32 v[64:65], %[ap] offset0:176 offset1:177\n" \
  "ds_read2_b32 v[66:67], %[ap] offset0:178 offset1:179\n" \
  "s_waitcnt lgkmcnt(2)\n" \
  "v_mfma_f32_32x32x16_bf16 %[a1], v[80:83], v[92:95], %[a1]\n" \
  "ds_read_b128 v[76:79], %[bp] offset:880\n" \
  "ds_read_b128 v[72:75], %[bp] offset:3440\n" \
  "v_mfma_f32_32x32x16_bf16 %[a0], v[80:83], v[88:91], %[a0]\n" \
  "ds_read2_b32 v[68:69], %[aq] offset0:176 offset1:177\n" \
  "ds_read2_b32 v[70:71], %[aq] offset0:178 offset1:179\n" \
  "v_mfma_f32_32x32x16_bf16 %[a1], v[84:87], v[88:91], %[a1]\n" \
  "ds_read2_b32 v[80:81], %[ap] offset0:184 offset1:185\n" \
  "ds_read2_b32 v[82:83], %[ap] offset0:186 offset1:187\n" \
  "s_waitcnt lgkmcnt(2)\n" \
  "v_mfma_f32_32x32x16_bf16 %[a1], v[64:67], v[76:79], %[a1]\n" \
  "ds_read_b128 v[92:95], %[bp] offset:912\n" \
  "ds_read_b128 v[88:91], %[bp] offset:3472\n" \
  "v_mfma_f32_32x32x16_bf16 %[a0], v[64:67], v[72:75], %[a0]\n" \
  "ds_read2_b32 v[84:85], %[aq] offset0:184 offset1:185\n" \
  "ds_read2_b32 v[86:87], %[aq] offset0:186 offset1:187\n" \
  "v_mfma_f32_32x32x16_bf16 %[a1], v[68:71], v[72:75], %[a1]\n" \
  "ds_read2_b32 v[64:65], %[ap] offset0:192 offset1:193\n" \
  "ds_read2_b32 v[66:67], %[ap] offset0:194 offset1:195\n" \
  "s_waitcnt lgkmcnt(2)\n" \
  "v_mfma_f32_32x32x16_bf16 %[a1], v[80:83], v[92:95], %[a1]\n" \
  "ds_read_b128 v[76:79], %[bp] offset:960\n" \
  "ds_read_b128 v[72:75], %[bp] offset:3520\n" \
  "v_mfma_f32_32x32x16_bf16 %[a0], v[80:83], v[88:91], %[a0]\n" \
  "ds_read2_b32 v[68:69], %[aq] offset0:192 offset1:193\n" \
  "ds_read2_b32 v[70:71], %[aq] offset0:194 offset1:195\n" \
  "v_mfma_f32_32x32x16_bf16 %[a1], v[84:87], v[88:91], %[a1]\n" \
  "ds_read2_b32 v[80:81], %[ap] offset0:200 offset1:201\n" \
  "ds_read2_b32 v[82:83], %[ap] offset0:202 offset1:203\n" \
  "s_waitcnt lgkmcnt(2)\n" \
  "v_mfma_f32_32x32x16_bf16 %[a1], v[64:67], v[76:79], %[a1]\n" \
  "ds_read_b128 v[92:95], %[bp] offset:992\n" \
  "ds_read_b128 v[88:91], %[bp] offset:3552\n" \
  "v_mfma_f32_32x32x16_bf16 %[a0], v[64:67], v[72:75], %[a0]\n" \
  "ds_read2_b32 v[84:85], %[aq] offset0:200 offset1:201\n" \
  "ds_read2_b32 v[86:87], %[aq] offset0:202 offset1:203\n" \
  "v_mfma_f32_32x32x16_bf16 %[a1], v[68:71], v[72:75], %[a1]\n" \
  "ds_read2_b32 v[64:65], %[ap] offset0:208 offset1:209\n" \
  "ds_read2_b32 v[66:67], %[ap] offset0:210 offset1:211\n" \
  "s_waitcnt lgkmcnt(2)\n" \
  "v_mfma_f32_32x32x16_bf16 %[a1], v[80:83], v[92:95], %[a1]\n" \
  "ds_read_b128 v[76:79], %[bp] offset:1040\n" \
  "ds_read_b128 v[72:75], %[bp] offset:3600\n" \
  "v_mfma_f32_32x32x16_bf16 %[a0], v[80:83], v[88:91], %[a0]\n" \
  "ds_read2_b32 v[68:69], %[aq] offset0:208 offset1:209\n" \
  "ds_read2_b32 v[70:71], %[aq] offset0:210 offset1:211\n" \
  "v_mfma_f32_32x32x16_bf16 %[a1], v[84:87], v[88:91], %[a1]\n" \
  "ds_read2_b32 v[80:81], %[ap] offset0:216 offset1:217\n" \
  "ds_read2_b32 v[82:83], %[ap] offset0:218 offset1:219\n" \
  "s_waitcnt lgkmcnt(2)\n" \
  "v_mfma_f32_32x32x16_bf16 %[a1], v[64:67], v[76:79], %[a1]\n" \
  "ds_read_b128 v[92:95], %[bp] offset:1072\n" \
  "ds_read_b128 v[88:91], %[bp] offset:3632\n" \
  "v_mfma_f32_32x32x16_bf16 %[a0], v[64:67], v[72:75], %[a0]\n" \
  "ds_read2_b32 v[84:85], %[aq] offset0:216 offset1:217\n" \
  "ds_read2_b32 v[86:87], %[aq] offset0:218 offset1:219\n" \
  "v_mfma_f32_32x32x16_bf16 %[a1], v[68:71], v[72:75], %[a1]\n" \
  "ds_read2_b32 v[64:65], %[ap] offset0:224 offset1:225\n" \
  "ds_read2_b32 v[66:67], %[ap] offset0:226 offset1:227\n" \
  "s_waitcnt lgkmcnt(2)\n" \
  "v_mfma_f32_32x32x16_bf16 %[a1], v[80:83], v[92:95], %[a1]\n" \
  "ds_read_b128 v[76:79], %[bp] offset:1120\n" \
  "ds_read_b128 v[72:75], %[bp] offset:3680\n" \
  "v_mfma_f32_32x32x16_bf16 %[a0], v[80:83], v[88:91], %[a0]\n" \
  "ds_read2_b32 v[68:69], %[aq] offset0:224 offset1:225\n" \
  "ds_read2_b32 v[70:71], %[aq] offset0:226 offset1:227\n" \
  "v_mfma_f32_32x32x16_bf16 %[a1], v[84:87], v[88:91], %[a1]\n" \
  "ds_read2_b32 v[80:81], %[ap] offset0:232 offset1:233\n" \
  "ds_read2_b32 v[82:83], %[ap] offset0:234 offset1:235\n" \
  "s_waitcnt lgkmcnt(2)\n" \
  "v_mfma_f32_32x32x16_bf16 %[a1], v[64:67], v[76:79], %[a1]\n" \
  "ds_read_b128 v[92:95], %[bp] offset:1152\n" \
  "ds_read_b128 v[88:91], %[bp] offset:3712\n" \
  "v_mfma_f32_32x32x16_bf16 %[a0], v[64:67], v[72:75], %[a0]\n" \
  "ds_read2_b32 v[84:85], %[aq] offset0:232 offset1:233\n" \
  "ds_read2_b32 v[86:87], %[aq] offset0:234 offset1:235\n" \
  "v_mfma_f32_32x32x16_bf16 %[a1], v[68:71], v[72:75], %[a1]\n" \
  "ds_read2_b32 v[64:65], %[ap] offset0:240 offset1:241\n" \
  "ds_read2_b32 v[66:67], %[ap] offset0:242 offset1:243\n" \
  "s_waitcnt lgkmcnt(2)\n" \
  "v_mfma_f32_32x32x16_bf16 %[a1], v[80:83], v[92:95], %[a1]\n" \
  "ds_read_b128 v[76:79], %[bp] offset:1200\n" \
  "ds_read_b128 v[72:75], %[bp] offset:3760\n" \
  "v_mfma_f32_32x32x16_bf16 %[a0], v[80:83], v[88:91], %[a0]\n" \
  "ds_read2_b32 v[68:69], %[aq] offset0:240 offset1:241\n" \
  "ds_read2_b32 v[70:71], %[aq] offset0:242 offset1:243\n" \
  "v_mfma_f32_32x32x16_bf16 %[a1], v[84:87], v[88:91], %[a1]\n" \
  "ds_read2_b32 v[80:81], %[ap] offset0:248 offset1:249\n" \
  "ds_read2_b32 v[82:83], %[ap] offset0:250 offset1:251\n" \
  "s_waitcnt lgkmcnt(2)\n" \
  "v_mfma_f32_32x32x16_bf16 %[a1], v[64:67], v[76:79], %[a1]\n" \
  "ds_read_b128 v[92:95], %[bp] offset:1232\n" \
  "ds_read_b128 v[88:91], %[bp] offset:3792\n" \
  "v_mfma_f32_32x32x16_bf16 %[a0], v[64:67], v[72:75], %[a0]\n" \
  "ds_read2_b32 v[84:85], %[aq] offset0:248 offset1:249\n" \
  "ds_read2_b32 v[86:87], %[aq] offset0:250 offset1:251\n" \
  "v_mfma_f32_32x32x16_bf16 %[a1], v[68:71], v[72:75], %[a1]\n" \
  "s_waitcnt lgkmcnt(0)\n" \
  "v_mfma_f32_32x32x16_bf16 %[a1], v[80:83], v[92:95], %[a1]\n" \
  "v_mfma_f32_32x32x16_bf16 %[a0], v[80:83], v[88:91], %[a0]\n" \
  "v_mfma_f32_32x32x16_bf16 %[a1], v[84:87], v[88:91], %[a1]\n" \
  "s_nop 15\n" \
  "s_nop 7\n"
#define CONV_LOOP_CLOBBERS "v64", "v65", "v66", "v67", "v68", "v69", "v70", "v71", "v72", "v73", "v74", "v75", "v76", "v77", "v78", "v79", "v80", "v81", "v82", "v83", "v84", "v85", "v86", "v87", "v88", "v89", "v90", "v91", "v92", "v93", "v94", "v95"
constexpr int AT_K = 0, AT_V = 65536;
constexpr int CV_H = 131072, CV_HB = 8320, CV_U = CV_H + 2 * CV_HB, CV_UB = 95 * 80  , LDS_BYTES = 163008;
constexpr int P3_WO = 49152, P3_T = 131072;
__device__ __forceinline__ constexpr int p3_o(int m) { return m < 48 ? m * 1024 : 114688 + (m - 48) * 1024; }

__device__ __forceinline__ void issue_wo(const Params& p, LAS char* lds, int wid, int lane) {
    const int ct = (blockIdx.x >> 3) & 7;
    const srd_t sw = mksrd(p.wo, D * D * 2);
    const unsigned wv = (lane >> 3) * 1024 + ((((lane & 7) >> 2) ^ ((lane >> 4) & 1)) * 64) + (lane & 3) * 16 + ct * 128;
    const unsigned l0 = (unsigned)(uintptr_t)lds + P3_WO;
#pragma unroll
    for (int i = 0; i < 8; ++i) { const int m = wid * 8 + i; dma16(sw, wv, m * 8192, l0 + m * 1024); }
}

__device__ __forceinline__ void phase2(const Params& p, LAS char* lds, unsigned long long tag) {
    char* ws = p.ws;
    const int tid = threadIdx.x, lane = tid & 63;
    const int wid = __builtin_amdgcn_readfirstlane(tid >> 6);
    const int bid = blockIdx.x, xc = bid & 7, jj = bid >> 3;
    const int i32 = lane & 31, hh = lane >> 5;
    const srd_t srd = mksrd(ws, WS_BYTES);
    STAMP(1, 0);
    if (wid >= 8) {
        const int cw = wid - 8, c2 = cw >> 1, half = cw & 1, ch = (xc * 32 + jj) * 2 + c2;
        LAS char* hb = lds + CV_H + c2 * CV_HB;
        LAS char* ub = lds + CV_U + c2 * CV_UB;
        const int t128 = half * 64 + lane;
        const u16* hrow = p.ssk + (size_t)ch * S;
        u32x4 hv[2], uv[2]; unsigned prev[2];
#pragma unroll
        for (int i = 0; i < 2; ++i) { const int t = t128 + 128 * i;
            hv[i] = *(const u32x4*)(hrow + 8 * t); prev[i] = t > 0 ? (unsigned)hrow[8 * t - 1] : 0u;
            if (!FUSED) uv[i] = ld16(srd, WS_UR + (ch * S + 8 * t) * 2); }
        const float dd = bflo((unsigned)p.ssd[ch]);
        BAR();
        if (FUSED && tid == 512) put_flag(ws, WS_F1B + ((bid & 7) * 32 + (bid >> 3)) * 8, tag);
        BAR(); BAR();
        if (FUSED) {
            wait_flags(ws, WS_F1B + xc * 256, 32, tag);
#pragma unroll
            for (int i = 0; i < 2; ++i) uv[i] = ld16(srd, WS_UR + (ch * S + 8 * (t128 + 128 * i)) * 2);
        }
#pragma unroll
        for (int i = 0; i < 2; ++i) { const int t = t128 + 128 * i;
            *(LAS u32x4*)(hb + (16 + 4 * t) * 4) = hv[i];
            u32x4 sv = {(hv[i][0] << 16) | prev[i], __builtin_amdgcn_alignbit(hv[i][1], hv[i][0], 16), __builtin_amdgcn_alignbit(hv[i][2], hv[i][1], 16), __builtin_amdgcn_alignbit(hv[i][3], hv[i][2], 16)};
            *(LAS u32x4*)(hb + 4160 + (16 + 4 * t) * 4) = sv;
            *(LAS u32x4*)(ub + (t >> 2) * 80 + (t & 3) * 16) = uv[i]; }
        if (t128 < 16) { *(LAS unsigned*)(hb + t128 * 4) = 0u; *(LAS unsigned*)(hb + 4160 + t128 * 4) = 0u; }
        for (int i = t128; i < 31 * 5; i += 128) *(LAS u32x4*)(ub + 64 * 80 + i * 16) = u32x4{0u, 0u, 0u, 0u};
        WAITL0(); BAR();
        STAMP(1, 1);
        const int r = i32;
        f32x16 acc0 = {}, acc1 = {};
        {
            const unsigned ap = (unsigned)(uintptr_t)hb + ((r & 1) ? 0 : 4160) + (((r + 2) >> 1) + 4 * hh) * 4 + half * 16 * 64;
            const unsigned bp = (unsigned)(uintptr_t)ub + (31 - r) * 80 + 16 * hh + half * 16 * 80;
            asm volatile(CONV_LOOP_ASM : [a0] "+v"(acc0), [a1] "+v"(acc1) : [ap] "v"(ap), [aq] "v"(ap + 2048), [bp] "v"(bp) : CONV_LOOP_CLOBBERS, "memory");
        }
        STAMP(1, 2);
        BAR();
        {
            const f32x16& snd = half ? acc0 : acc1;
            LAS char* px = lds + 32768 + cw * 4096 + lane * 16;
#pragma unroll
            for (int g = 0; g < 4; ++g) *(LAS f32x4*)(px + g * 1024) = f32x4{snd[4 * g], snd[4 * g + 1], snd[4 * g + 2], snd[4 * g + 3]};
        }
        WAITL0(); BAR();
        {
            const f32x16& own = half ? acc1 : acc0;
            LAS const char* px = lds + 32768 + (cw ^ 1) * 4096 + lane * 16;
            const int t0 = half * 1024;
            LAS const char* ue = ub + (63 - half * 32 - r) * 80;
            LAS char* zl = hb + half * 4096;
#pragma unroll
            for (int g = 0; g < 4; ++g) {
                const f32x4 o = *(LAS const f32x4*)(px + g * 1024);
                const int rr = 8 * g + 4 * hh;
                const u32x2 uu = *(LAS const u32x2*)(ue + (28 - rr) * 2);
                const float u3 = bflo(uu[0]), u2 = bfhi(uu[0]), u1 = bflo(uu[1]), u0 = bfhi(uu[1]);
                const float z0 = rbf(own[4 * g + 0] + o[0]) + rbf(u0 * dd), z1 = rbf(own[4 * g + 1] + o[1]) + rbf(u1 * dd);
                const float z2 = rbf(own[4 * g + 2] + o[2]) + rbf(u2 * dd), z3 = rbf(own[4 * g + 3] + o[3]) + rbf(u3 * dd);
                *(LAS u32x2*)(zl + (32 * r + rr) * 2) = u32x2{cvtpk(z0, z1), cvtpk(z2, z3)};
            }
            WAITL0();
#pragma unroll
            for (int i = 0; i < 2; ++i) st16(srd, WS_ZT + (ch * S + t0) * 2 + i * 1024 + lane * 16, *(LAS const u32x4*)(zl + i * 1024 + lane * 16));
        }
        STAMP(1, 4);
        if (FUSED) { WAITV(0); BAR(); }
        return;
    }
    const int head = xc * 4 + (jj >> 3), qb = jj & 7;
    const int qp = wid & 3, kh = wid >> 2;
    const int qt = qb * 8 + 2 * qp;
    if (wid == 0) {
        gu64* w = (gu64*)(ws + WS_F1A) + head * 64 + lane;
        unsigned long long v;
        for (unsigned spins = 0; spins < (1u << 16); ++spins) {
            v = __hip_atomic_load(w, __ATOMIC_RELAXED, __HIP_MEMORY_SCOPE_AGENT);
            if (!FUSED || __all((unsigned)(v >> 32) == (unsigned)tag)) break;
            __builtin_amdgcn_s_sleep(1);
        }
        const float nv = __uint_as_float((unsigned)v);
        float kn = (lane & 1) ? 0.f : nv, qn = ((lane & 1) && (lane >> 3) == qb) ? nv : 0.f;
        kn = dppmax<0x143, 0xc>(dppmax<0x142, 0xa>(rowmax16(kn))); qn = dppmax<0x143, 0xc>(dppmax<0x142, 0xa>(rowmax16(qn)));
        if (lane == 63) *(LAS int*)(lds + FAST_LDS) = (kn * qn * 1.02f <= FAST_BOUND2) ? 1 : 0;
        WAITL0();
    }
    BAR();
    STAMP(1, 5);
    bf16x8 q1, q2;
    {
        const unsigned oq = WS_QF + ((head * 64 + qt) * 64 + lane) * 16;
        asm volatile("buffer_load_dwordx4 %0, %2, %3, 0 offen" SC1 "\n\tbuffer_load_dwordx4 %1, %2, %3, 0 offen offset:1024" SC1 : "=&v"(q1), "=&v"(q2) : "v"(oq), "s"(srd) : "memory");
    }
    {
        const unsigned voff = wid * 1024 + lane * 16;
        const unsigned l0 = (unsigned)(uintptr_t)lds + wid * 1024;
        const unsigned kb = WS_KF + head * 65536, vb = WS_VF + head * 65536;
#pragma unroll
        for (int i = 0; i < 8; ++i) {
            const int c = (i >> 1) + (i & 1) * 4;
            dma16c(srd, voff, kb + c * 8192, l0 + AT_K + c * 8192);
            dma16c(srd, voff, vb + c * 8192, l0 + AT_V + c * 8192);
        }
    }
    const bool fast = __builtin_amdgcn_readfirstlane(*(LAS const int*)(lds + FAST_LDS)) != 0;
    const unsigned fill = (i32 == 16 || i32 == 20) ? 0x3F803F80u : 0u;
    f32x16 o1 = {}, o2 = {};
    STAMP(1, 1); STAMPC(1, 1);
    if (fast) {
        unsigned ka = (unsigned)(uintptr_t)lds + AT_K + kh * 32768 + lane * 16;
        unsigned va = (unsigned)(uintptr_t)lds + AT_V + kh * 32768 + hh * 256 + (i32 & 15) * 16;
        unsigned g;
        const unsigned long long vm = 0x0000FFFF0000FFFFull;
        asm volatile(ATTN_LOOP_ASM : [o1] "+v"(o1), [o2] "+v"(o2), [ka] "+v"(ka), [va] "+v"(va), [g] "=&s"(g), [q1] "+v"(q1), [q2] "+v"(q2)
                     : [vm] "s"(vm), [fill] "v"(fill), [w0] "n"(12), [w1] "n"(8) : ATTN_LOOP_CLOBBERS, "scc", "memory");
    } else {
        asm volatile("s_waitcnt vmcnt(0)" : "+v"(q1), "+v"(q2) :: "memory"); BAR(); BAR(); BAR();
        const bool real = i32 < 16;
        const bf16x8 fl = __builtin_bit_cast(bf16x8, u32x4{fill, fill, fill, fill});
        float m1 = -INFINITY, m2 = -INFINITY;
        for (int kt = 0; kt < 64; ++kt) {
            const bf16x8 kf = *(LAS const bf16x8*)(lds + AT_K + kt * 1024 + lane * 16);
            const f32x16 s1 = MFMA32(kf, q1, (f32x16){}), s2 = MFMA32(kf, q2, (f32x16){});
#pragma unroll
            for (int r = 0; r < 16; ++r) { m1 = __builtin_fmaxf(m1, s1[r]); m2 = __builtin_fmaxf(m2, s2[r]); }
        }
        m1 = __builtin_fmaxf(m1, swap32(m1)); m2 = __builtin_fmaxf(m2, swap32(m2));
        if (m1 == -INFINITY) m1 = 0.f;
        if (m2 == -INFINITY) m2 = 0.f;
        for (int kt = kh * 32; kt < kh * 32 + 32; ++kt) {
            const bf16x8 kf = *(LAS const bf16x8*)(lds + AT_K + kt * 1024 + lane * 16);
            bf16x8 v0 = *(LAS const bf16x8*)(lds + AT_V + kt * 1024 + hh * 256 + (i32 & 15) * 16);
            bf16x8 v1 = *(LAS const bf16x8*)(lds + AT_V + kt * 1024 + 512 + hh * 256 + (i32 & 15) * 16);
            v0 = real ? v0 : fl; v1 = real ? v1 : fl;
            f32x16 s1 = MFMA32(kf, q1, (f32x16){}), s2 = MFMA32(kf, q2, (f32x16){});
#pragma unroll
            for (int r = 0; r < 16; ++r) { s1[r] = __builtin_amdgcn_exp2f(s1[r] - m1); s2[r] = __builtin_amdgcn_exp2f(s2[r] - m2); }
            o1 = smf(v0, lo8(s1), o1); o1 = smf(v1, hi8(s1), o1);
            o2 = smf(v0, lo8(s2), o2); o2 = smf(v1, hi8(s2), o2);
        }
    }
    STAMP(1, 3); STAMPC(1, 3);
    BAR();
    {
        const f32x16& snd = kh ? o1 : o2;
        LAS char* px = lds + wid * 4096 + lane * 16;
        *(LAS f32x4*)(px) = f32x4{snd[0], snd[1], snd[2], snd[3]};
        *(LAS f32x4*)(px + 1024) = f32x4{snd[4], snd[5], snd[6], snd[7]};
        *(LAS float*)(lds + wid * 4096 + 2048 + lane * 4) = snd[8];
    }
    WAITL0(); BAR();
    {
        const f32x16& own = kh ? o2 : o1;
        LAS const char* px = lds + (wid ^ 4) * 4096 + lane * 16;
        const f32x4 a = *(LAS const f32x4*)(px), b = *(LAS const f32x4*)(px + 1024);
        const float l = own[8] + *(LAS const float*)(lds + (wid ^ 4) * 4096 + 2048 + lane * 4);
        const float inv = 1.0f / l;
        st16(srd, WS_OF + (((qt + kh) * 32 + head) * 64 + lane) * 16,
             u32x4{cvtpk((own[0] + a[0]) * inv, (own[1] + a[1]) * inv), cvtpk((own[2] + a[2]) * inv, (own[3] + a[3]) * inv),
                   cvtpk((own[4] + b[0]) * inv, (own[5] + b[1]) * inv), cvtpk((own[6] + b[2]) * inv, (own[7] + b[3]) * inv)});
    }
    STAMP(1, 4);
    if (FUSED) {
        issue_wo(p, lds, wid, lane);
        WAITV(8); BAR();
        if (tid == 0) put_flag(ws, WS_F2A + (qb * 32 + head) * 8, tag);
        if (tid == 1) put_flag(ws, WS_F2B + (xc * 32 + jj) * 8, tag);
    }
}

__device__ __forceinline__ void phase3(const Params& p, LAS char* lds, unsigned long long tag) {
    char* ws = p.ws;
    const int tid = threadIdx.x, lane = tid & 63;
    const int wid = __builtin_amdgcn_readfirstlane(tid >> 6);
    const int bid = blockIdx.x, xc = bid & 7, jj = bid >> 3;
    const srd_t srd = mksrd(ws, WS_BYTES);
    const int rt = xc * 4 + (jj >> 3), ct = jj & 7;
    STAMP(2, 0);
    if (wid >= 8) {
        const int t = tid - 512, st = rt, dt = ct;
        LAS u16* tl = (LAS u16*)(lds + P3_T);
        u32x4 z[2], qv[2];
#pragma unroll
        for (int i = 0; i < 2; ++i) { const int e = t + 256 * i, r = e >> 3, c = e & 7; qv[i] = *(const u32x4*)(p.q + (size_t)(st * 64 + r) * D + dt * 64 + c * 8); }
        if (FUSED) { if (wid == 8) wait_flags(ws, WS_F2B + dt * 256, 32, tag); BAR(); }
#pragma unroll
        for (int i = 0; i < 2; ++i) { const int e = t + 256 * i, r = e >> 3, c = e & 7; z[i] = ld16(srd, WS_ZT + ((dt * 64 + r) * S + st * 64 + c * 8) * 2); }
#pragma unroll
        for (int i = 0; i < 2; ++i) { const int e = t + 256 * i, r = e >> 3, c = e & 7; *(LAS u32x4*)(tl + r * 72 + c * 8) = z[i]; }
        WAITL0(); BAR();
#pragma unroll
        for (int i = 0; i < 2; ++i) { const int e = t + 256 * i, s = e >> 3, c = e & 7;
            u32x4 ov;
#pragma unroll
            for (int j = 0; j < 4; ++j) { const float z0 = bflo((unsigned)tl[(c * 8 + 2 * j) * 72 + s]), z1 = bflo((unsigned)tl[(c * 8 + 2 * j + 1) * 72 + s]);
                ov[j] = cvtpk(bflo(qv[i][j]) * z0, bfhi(qv[i][j]) * z1); }
            *(u32x4*)(p.out + (size_t)(st * 64 + s) * (2 * D) + dt * 64 + c * 8) = ov; }
        STAMP(2, 3);
        return;
    }
    if (FUSED) {
        if (wid == 0) wait_flags(ws, WS_F2A + (rt >> 2) * 256, 32, tag);
        BAR();
        STAMP(2, 4);
        if (bid == 0 && tid == 64) put_flag(ws, WS_EPOCH, tag);
    } else issue_wo(p, lds, wid, lane);
    {
        const unsigned l0 = (unsigned)(uintptr_t)lds;
#pragma unroll
        for (int i = 0; i < 8; ++i) { const int m = wid * 8 + i;
            dma16c(srd, lane * 16, WS_OF + (rt * 2 * 32 + m) * 1024, l0 + (m < 48 ? m * 1024 : 114688 + (m - 48) * 1024)); }
    }
    WAITV(0); BAR();
    STAMP(2, 1);
    if (wid >= 4) return;
    const int wr = wid >> 1, wc = wid & 1, hh = lane >> 5, qrow = (lane & 15) >> 2;
    LAS const char* wp = lds + P3_WO + (4 * hh + qrow) * 128 + ((wc ^ (qrow >> 1)) * 64) + ((lane >> 4) & 1) * 32 + (lane & 3) * 8;
    LAS const char* opa = lds + wr * 32768 + lane * 16;
    LAS const char* opb = lds + (wr ? 114688 : 16384) + lane * 16;
    f32x16 acc = {}, acc2 = {};
#pragma unroll
    for (int h = 0; h < 16; ++h) {
        const bf16x8 wf = cat8(trread(wp + h * 2048), trread(wp + h * 2048 + 1024)), of = *(LAS const bf16x8*)(opa + h * 1024);
        const bf16x8 wf2 = cat8(trread(wp + 32768 + h * 2048), trread(wp + 32768 + h * 2048 + 1024)), of2 = *(LAS const bf16x8*)(opb + h * 1024);
        acc = MFMA32(wf, of, acc);
        acc2 = MFMA32(wf2, of2, acc2);
    }
    STAMP(2, 2);
    unsigned pk[8];
#pragma unroll
    for (int j = 0; j < 8; ++j) pk[j] = cvtpk(acc[2 * j] + acc2[2 * j], acc[2 * j + 1] + acc2[2 * j + 1]);
    u16* orow = p.out + (size_t)((rt * 2 + wr) * 32 + (lane & 31)) * (2 * D) + D + ct * 64 + wc * 32 + hh * 8;
#pragma unroll
    for (int g = 0; g < 4; g += 2) {
        auto s0 = __builtin_amdgcn_permlane32_swap(pk[2 * g], pk[2 * g + 2], false, false);
        auto s1 = __builtin_amdgcn_permlane32_swap(pk[2 * g + 1], pk[2 * g + 3], false, false);
        *(u32x4*)(orow + g * 8) = u32x4{s0[0], s1[0], s0[1], s1[1]};
    }
    STAMP(2, 3);
}

__global__ __launch_bounds__(768, 3) void k_all(Params p) {
    extern __shared__ __attribute__((aligned(16))) char smem[];
    unsigned long long tag = *(const unsigned long long*)(p.ws + WS_EPOCH) + 1ull;
    asm volatile("" : "+s"(tag));
    phase1(p, (LAS char*)smem, tag);
    phase2(p, (LAS char*)smem, tag);
    phase3(p, (LAS char*)smem, tag);
}

extern "C" void kernel_launch(void* const* d_in, const int* in_sizes, int n_in, void* d_out, int out_size, void* d_ws, size_t ws_size, hipStream_t stream) {
    Params p;
    p.q = (const u16*)d_in[0]; p.k = (const u16*)d_in[1]; p.v = (const u16*)d_in[2];
    p.shk = (const u16*)d_in[3]; p.ssk = (const u16*)d_in[4]; p.ssd = (const u16*)d_in[5];
    p.x = (const u16*)d_in[6]; p.wqkv = (const u16*)d_in[7]; p.wo = (const u16*)d_in[8];
    p.out = (u16*)d_out; p.ws = (char*)d_ws;
    (void)hipFuncSetAttribute((const void*)k_all, hipFuncAttributeMaxDynamicSharedMemorySize, LDS_BYTES);
    k_all<<<256, 768, LDS_BYTES, stream>>>(p);
}
```

```cpp
#include <hip/hip_runtime.h>
#include <hip/hip_bf16.h>
#include <stdint.h>

typedef __bf16 bf16x8 __attribute__((ext_vector_type(8)));
typedef __bf16 bf16x16 __attribute__((ext_vector_type(16)));
typedef short s16x4 __attribute__((ext_vector_type(4)));
typedef float f32x16 __attribute__((ext_vector_type(16)));
typedef float f32x8 __attribute__((ext_vector_type(8)));
typedef float f32x4 __attribute__((ext_vector_type(4)));
typedef unsigned u32x4 __attribute__((ext_vector_type(4)));
typedef unsigned u32x2 __attribute__((ext_vector_type(2)));
typedef unsigned short u16;
#define LAS __attribute__((address_space(3)))
#define GAS __attribute__((address_space(1)))
typedef __amdgpu_buffer_rsrc_t srd_t;

constexpr int S = 2048, D = 512;
constexpr float QSCALE = 0.25f * 1.4426950408889634f;
constexpr float FAST_BOUND2 = 60.f * 60.f;

constexpr unsigned WS_QF = 0, WS_KF = 2u << 20, WS_VF = 4u << 20, WS_OF = 6u << 20, WS_UR = 8u << 20, WS_ZT = 10u << 20,
                   WS_F1A = 12u << 20,
                   WS_F1B = WS_F1A + 65536,
                   WS_F2A = WS_F1B + 65536,
                   WS_F2B = WS_F2A + 65536,
                   WS_EPOCH = WS_F2B + 65536,
                   WS_BYTES = 13u << 20;
#define FUSED 1
#define POLL_SLEEP 4
#define CONV_PRIO 0
constexpr int AUX = FUSED ? 16 : 0;
#define SC1 " sc1"

#define STAMP(kid, slot) do {} while (0)
#define STAMPC(kid, slot) do {} while (0)
#define TS(i) do {} while (0)
#define TS_DECL do {} while (0)
#define TS_FLUSH(kid) do {} while (0)
#define MFMA32(a, b, c) __builtin_amdgcn_mfma_f32_32x32x16_bf16(a, b, c, 0, 0, 0)
#define BAR() asm volatile("s_barrier" ::: "memory")
#define WAITV(n) asm volatile("s_waitcnt vmcnt(" #n ")" ::: "memory")
#define WAITL0() asm volatile("s_waitcnt lgkmcnt(0)" ::: "memory")
#define SB() __builtin_amdgcn_sched_barrier(0)

__device__ __forceinline__ unsigned cvtpk(float lo, float hi) {
    typedef __bf16 bf2 __attribute__((ext_vector_type(2)));
    bf2 r; r[0] = (__bf16)lo; r[1] = (__bf16)hi; return __builtin_bit_cast(unsigned, r);
}
__device__ __forceinline__ float bflo(unsigned u) { return __uint_as_float(u << 16); }
__device__ __forceinline__ float bfhi(unsigned u) { return __uint_as_float(u & 0xffff0000u); }
__device__ __forceinline__ float rbf(float f) { return __uint_as_float(cvtpk(f, 0.f) << 16); }
__device__ __forceinline__ s16x4 trread(LAS const char* p) {
    typedef short v4i16_t __attribute__((ext_vector_type(4)));
    return __builtin_amdgcn_ds_read_tr16_b64_v4i16((LAS v4i16_t*)p);
}
__device__ __forceinline__ bf16x8 cat8(s16x4 a, s16x4 b) {
    typedef short s16x8 __attribute__((ext_vector_type(8)));
    s16x8 r = __builtin_shufflevector(a, b, 0, 1, 2, 3, 4, 5, 6, 7);
    return __builtin_bit_cast(bf16x8, r);
}
__device__ __forceinline__ f32x16 smf(bf16x8 a, f32x8 b, f32x16 c) {
    return __builtin_amdgcn_smfmac_f32_32x32x32_bf16(a, __builtin_bit_cast(bf16x16, b), c, 0xDDDD, 0, 0);
}
__device__ __forceinline__ f32x8 lo8(f32x16 x) { return __builtin_shufflevector(x, x, 0, 1, 2, 3, 4, 5, 6, 7); }
__device__ __forceinline__ f32x8 hi8(f32x16 x) { return __builtin_shufflevector(x, x, 8, 9, 10, 11, 12, 13, 14, 15); }
__device__ __forceinline__ float swap32(float x) {
    auto r = __builtin_amdgcn_permlane32_swap(__float_as_uint(x), __float_as_uint(x), false, false);
    return __uint_as_float((threadIdx.x & 32) ? r[0] : r[1]);
}
template <int CTRL, int RM> __device__ __forceinline__ float dppmax(float v) {
    return __builtin_fmaxf(v, __builtin_bit_cast(float, __builtin_amdgcn_update_dpp(0, __builtin_bit_cast(int, v), CTRL, RM, 0xf, true)));
}
__device__ __forceinline__ float rowmax16(float v) { v = dppmax<0x111, 0xf>(v); v = dppmax<0x112, 0xf>(v); v = dppmax<0x114, 0xf>(v); return dppmax<0x118, 0xf>(v); }
__device__ __forceinline__ void dma16(srd_t srd, unsigned voff, unsigned soff, unsigned ldsaddr) {
    asm volatile("s_mov_b32 m0, %0\n\ts_nop 0\n\tbuffer_load_dwordx4 %1, %2, %3 offen lds" :: "s"(ldsaddr), "v"(voff), "s"(srd), "s"(soff) : "memory");
}
__device__ __forceinline__ void dma16c(srd_t srd, unsigned voff, unsigned soff, unsigned ldsaddr) {
    asm volatile("s_mov_b32 m0, %0\n\ts_nop 0\n\tbuffer_load_dwordx4 %1, %2, %3 offen" SC1 " lds" :: "s"(ldsaddr), "v"(voff), "s"(srd), "s"(soff) : "memory");
}
__device__ __forceinline__ void st16(srd_t srd, unsigned off, u32x4 v) { __builtin_amdgcn_raw_buffer_store_b128(v, srd, off, 0, AUX); }
__device__ __forceinline__ void st8(srd_t srd, unsigned off, u32x2 v) { __builtin_amdgcn_raw_buffer_store_b64(v, srd, off, 0, AUX); }
__device__ __forceinline__ void st4(srd_t srd, unsigned off, float v) { __builtin_amdgcn_raw_buffer_store_b32(__float_as_uint(v), srd, off, 0, AUX); }
__device__ __forceinline__ u32x4 ld16(srd_t srd, unsigned off) { return __builtin_amdgcn_raw_buffer_load_b128(srd, off, 0, AUX); }
typedef GAS unsigned long long gu64;
template <int SH> __device__ __forceinline__ unsigned long long poll_block(char* ws, unsigned off, unsigned long long tag) {
    gu64* w = (gu64*)(ws + off) + (threadIdx.x & 31);
    const unsigned long long want = (tag << SH) >> SH;
    unsigned long long v = 0;
    for (unsigned spins = 0; spins < (1u << 16); ++spins) {
        v = __hip_atomic_load(w, __ATOMIC_RELAXED, __HIP_MEMORY_SCOPE_AGENT);
        if (__all((v >> SH) == want)) break;
        __builtin_amdgcn_s_sleep(POLL_SLEEP);
    }
    asm volatile("" ::: "memory");
    return v;
}
__device__ __forceinline__ unsigned code12(float v) { const unsigned u = (__float_as_uint(v) + 0x7ffffu) >> 19; return u > 0xff0u ? 0xff0u : u; }
__device__ __forceinline__ float decode12(unsigned c) { return __uint_as_float(c << 19); }
__device__ __forceinline__ unsigned lds_inc(LAS char* lds, int off) {
    unsigned old = 0;
    if ((threadIdx.x & 63) == 0) old = __hip_atomic_fetch_add((LAS unsigned*)(lds + off), 1u, __ATOMIC_RELAXED, __HIP_MEMORY_SCOPE_WORKGROUP);
    return __builtin_amdgcn_readfirstlane(old);
}
__device__ __forceinline__ void lds_wait(LAS char* lds, int off, unsigned want) {
    for (unsigned spins = 0; spins < (1u << 22); ++spins) {
        if (__builtin_amdgcn_readfirstlane(__hip_atomic_load((LAS unsigned*)(lds + off), __ATOMIC_RELAXED, __HIP_MEMORY_SCOPE_WORKGROUP)) >= want) break;
        __builtin_amdgcn_s_sleep(1);
    }
    asm volatile("" ::: "memory");
}
__device__ __forceinline__ void put_flag(char* ws, unsigned off, unsigned long long tag) { __hip_atomic_store((gu64*)(ws + off), tag, __ATOMIC_RELAXED, __HIP_MEMORY_SCOPE_AGENT); }
__device__ __forceinline__ unsigned long long load_tag(const char* ws) {
    unsigned long long e;
    asm volatile("s_load_dwordx2 %0, %1, 0x0\n\ts_waitcnt lgkmcnt(0)" : "=&s"(e) : "s"(ws + WS_EPOCH) : "memory");
    return e + 1ull;
}
__device__ __forceinline__ srd_t mksrd(const void* p, unsigned bytes) { return __builtin_amdgcn_make_buffer_rsrc((void*)p, (short)0, (int)bytes, 0x00020000); }

struct Params {
    const u16 *q, *k, *v, *shk, *ssk, *ssd, *x, *wqkv, *wo;
    u16* out; char* ws;
};

constexpr int P1_STAGE = 32768, P1_NST = 4, NRM_LDS = 162912, FAST_LDS = NRM_LDS + 64;
constexpr int CNT_QKV = FAST_LDS + 4, CNT_O = FAST_LDS + 8, CNT_Z = FAST_LDS + 12, CNT_GO = FAST_LDS + 16, CNT_LAND = FAST_LDS + 20, CNT_U = FAST_LDS + 24, CNT_ZGO = FAST_LDS + 28,
              CNT_GO1 = FAST_LDS + 32, CNT_KV = FAST_LDS + 36, CNT_ADONE = FAST_LDS + 40, CNT_APART = FAST_LDS + 44, CNT_T1 = FAST_LDS + 48,
              CNT_CB = FAST_LDS + 52, CNT_CDONE = FAST_LDS + 60, CNT_CPART = FAST_LDS + 68, N_CNT = 20;

__device__ __forceinline__ void phase1(const Params& p, LAS char* lds, unsigned long long& tag) {
    char* ws = p.ws;
    const int tid = threadIdx.x, lane = tid & 63;
    const int wid = __builtin_amdgcn_readfirstlane(tid >> 6);
    const int bid = blockIdx.x, xc = bid & 7, jj = bid >> 3;
    const int rb = (xc & 3) * 8 + (jj & 7), g4 = (xc >> 2) * 4 + (jj >> 3);
    const int rot = (jj + 2 * xc) & 7;
    const srd_t sws = mksrd(ws, WS_BYTES);
    STAMP(0, 0);
    if (wid >= 8) {
        const int pw = wid - 8;
        const int slab = bid >> 3, cg = bid & 7;
        const int s0 = slab * 64 + (lane >> 3) * 8, ch = cg * 64 + pw * 16 + (lane & 7) * 2;
        if (FUSED) tag = load_tag(ws);
        BAR(); BAR(); BAR(); BAR(); BAR(); BAR(); BAR();
        BAR();
        lds_wait(lds, CNT_QKV, 4);
        unsigned kk[11], vv[8];
        const srd_t sk = mksrd(p.k, S * D * 2), sv = mksrd(p.v, S * D * 2);
        const int ko = ((s0 - 3) * D + ch) * 2;
#pragma unroll
        for (int i = 0; i < 11; ++i) kk[i] = __builtin_amdgcn_raw_buffer_load_b32(sk, (s0 - 3 + i >= 0) ? ko + i * D * 2 : 0x7ffffff0, 0, 0);
#pragma unroll
        for (int i = 0; i < 8; ++i) vv[i] = __builtin_amdgcn_raw_buffer_load_b32(sv, ((s0 + i) * D + ch) * 2, 0, 0);
        const u32x4 sw = *(const u32x4*)(p.shk + ch * 4);
        STAMP(0, 2);
        float w0[4], w1[4];
        w0[0] = bflo(sw[0]); w0[1] = bfhi(sw[0]); w0[2] = bflo(sw[1]); w0[3] = bfhi(sw[1]);
        w1[0] = bflo(sw[2]); w1[1] = bfhi(sw[2]); w1[2] = bflo(sw[3]); w1[3] = bfhi(sw[3]);
        float r0[8], r1[8];
#pragma unroll
        for (int i = 0; i < 8; ++i) {
            float a0 = bflo(kk[i]) * w0[3], a1 = bfhi(kk[i]) * w1[3];
            a0 += bflo(kk[i + 1]) * w0[2]; a1 += bfhi(kk[i + 1]) * w1[2];
            a0 += bflo(kk[i + 2]) * w0[1]; a1 += bfhi(kk[i + 2]) * w1[1];
            a0 += bflo(kk[i + 3]) * w0[0]; a1 += bfhi(kk[i + 3]) * w1[0];
            r0[i] = rbf(a0) * bflo(vv[i]); r1[i] = rbf(a1) * bfhi(vv[i]);
        }
        const unsigned uo = WS_UR + (ch * S + (S - 64 - slab * 64) + (7 - (lane >> 3)) * 8) * 2;
        st16(sws, uo, u32x4{cvtpk(r0[7], r0[6]), cvtpk(r0[5], r0[4]), cvtpk(r0[3], r0[2]), cvtpk(r0[1], r0[0])});
        st16(sws, uo + S * 2, u32x4{cvtpk(r1[7], r1[6]), cvtpk(r1[5], r1[4]), cvtpk(r1[3], r1[2]), cvtpk(r1[1], r1[0])});
        STAMP(0, 3);
        if (FUSED) { WAITV(0); if (lds_inc(lds, CNT_T1) == 3 && lane < 32) put_flag(ws, WS_F1B + (((bid & 7) * 32 + lane) * 32 + (bid >> 3)) * 8, tag); }
        return;
    }
    if (wid >= 4) {
        const int pw = wid - 4;
        const srd_t sx = mksrd(p.x, S * D * 2), sw = mksrd(p.wqkv, D * 3 * D * 2);
        unsigned xo[2], wv[2];
#pragma unroll
        for (int i = 0; i < 2; ++i) { const int row = 8 * (2 * pw + i) + (lane >> 3), c = (lane & 7) ^ ((row >> 1) & 7); xo[i] = ((rb * 64 + row) * D + c * 8) * 2;
            wv[i] = row * 3072 + ((((lane & 7) >> 2) ^ ((lane >> 4) & 1)) * 64) + (lane & 3) * 16 + g4 * 128; }
        const unsigned l0 = (unsigned)(uintptr_t)lds + pw * 2048;
        auto issue = [&](int c) {
            const unsigned st = l0 + (c % P1_NST) * P1_STAGE; const int cc = (c + rot) & 7;
            const unsigned sox = cc * 128, sow = cc * 196608;
            asm volatile("s_mov_b32 m0, %[st]\n\ts_nop 0\n\tbuffer_load_dwordx4 %[x0], %[sx], %[sox] offen lds\n\t"
                         "s_add_u32 m0, %[st], 1024\n\ts_nop 0\n\tbuffer_load_dwordx4 %[x1], %[sx], %[sox] offen lds\n\t"
                         "s_add_u32 m0, %[st], 8192\n\ts_nop 0\n\tbuffer_load_dwordx4 %[w0], %[sw], %[sow] offen lds\n\t"
                         "s_add_u32 m0, %[st], 9216\n\ts_nop 0\n\tbuffer_load_dwordx4 %[w1], %[sw], %[sow] offen lds\n\t"
                         "s_add_u32 m0, %[st], 15360\n\ts_nop 0\n\tbuffer_load_dwordx4 %[w0], %[sw], %[sow] offen offset:1024 lds\n\t"
                         "s_add_u32 m0, %[st], 16384\n\ts_nop 0\n\tbuffer_load_dwordx4 %[w1], %[sw], %[sow] offen offset:1024 lds\n\t"
                         "s_add_u32 m0, %[st], 22528\n\ts_nop 0\n\tbuffer_load_dwordx4 %[w0], %[sw], %[sow] offen offset:2048 lds\n\t"
                         "s_add_u32 m0, %[st], 23552\n\ts_nop 0\n\tbuffer_load_dwordx4 %[w1], %[sw], %[sow] offen offset:2048 lds"
                         :: [st] "s"(st), [x0] "v"(xo[0]), [x1] "v"(xo[1]), [w0] "v"(wv[0]), [w1] "v"(wv[1]), [sx] "s"(sx), [sw] "s"(sw), [sox] "s"(sox), [sow] "s"(sow) : "scc", "memory");
        };
        TS_DECL; TS(0);
        issue(0); issue(1); TS(1);
        if (FUSED) tag = load_tag(ws);
        WAITV(8); TS(2); BAR(); issue(2); issue(3); TS(3);
        WAITV(16); TS(4); BAR(); issue(4); TS(5);
        WAITV(16); TS(6); BAR(); issue(5); TS(7);
        WAITV(16); TS(8); BAR(); issue(6); TS(9);
        WAITV(16); TS(10); BAR(); issue(7); TS(11);
        WAITV(16); TS(12); BAR();
        WAITV(8); TS(13); BAR();
        WAITV(0); TS(14);
        BAR();
        TS_FLUSH(3);
        return;
    }
    if (FUSED) tag = load_tag(ws);
    const int i32 = lane & 31, hh = lane >> 5;
    const int tl = wid & 1, cf = wid >> 1;
    const int row = tl * 32 + i32, swz = (row >> 1) & 7;
    int xoff[4];
#pragma unroll
    for (int s = 0; s < 4; ++s) xoff[s] = row * 128 + (((2 * s + hh) ^ swz) * 16);
    const int qrow = (lane & 15) >> 2;
    const int woff = 8192 + (8 * hh + qrow) * 128 + ((cf ^ (qrow >> 1)) * 64) + ((lane >> 4) & 1) * 32 + (lane & 3) * 8;
    f32x16 aq = {}, ak = {}, av = {};
    bf16x8 xa[2], qa[2], ka[2], va[2], xb[2], qb[2], kb[2], vb[2];
#define P1_RD(X, Q, K, V, st, s0) do { _Pragma("unroll") for (int s_ = 0; s_ < 2; ++s_) { \
        LAS const char* wp_ = (st) + woff + ((s0) + s_) * 2048; \
        X[s_] = *(LAS const bf16x8*)((st) + xoff[(s0) + s_]); \
        Q[s_] = cat8(trread(wp_), trread(wp_ + 512)); K[s_] = cat8(trread(wp_ + 8192), trread(wp_ + 8192 + 512)); V[s_] = cat8(trread(wp_ + 16384), trread(wp_ + 16384 + 512)); } } while (0)
#define P1_MM(X, Q, K, V) do { _Pragma("unroll") for (int s_ = 0; s_ < 2; ++s_) { aq = MFMA32(Q[s_], X[s_], aq); ak = MFMA32(K[s_], X[s_], ak); av = MFMA32(X[s_], V[s_], av); } } while (0)
#define P1_MIX() do { _Pragma("unroll") for (int i_ = 0; i_ < 4; ++i_) { __builtin_amdgcn_sched_group_barrier(0x008, 1, 0); __builtin_amdgcn_sched_group_barrier(0x100, 3, 0); } \
        __builtin_amdgcn_sched_group_barrier(0x008, 1, 0); __builtin_amdgcn_sched_group_barrier(0x100, 2, 0); __builtin_amdgcn_sched_group_barrier(0x008, 1, 0); } while (0)
    TS_DECL;
#pragma unroll
    for (int c = 0; c < 8; ++c) {
        BAR();
        TS(c);
        LAS const char* st = lds + (c % P1_NST) * P1_STAGE;
        P1_RD(xa, qa, ka, va, st, 0);
        if (c > 0) { P1_MM(xb, qb, kb, vb); P1_MIX(); }
        SB();
        P1_RD(xb, qb, kb, vb, st, 2);
        P1_MM(xa, qa, ka, va); P1_MIX();
        SB();
        WAITL0();
    }
    TS(8);
    P1_MM(xb, qb, kb, vb);
    TS_FLUSH(3);
    STAMP(0, 9);
    const int tt = rb * 2 + tl;
    const int h0 = g4 * 4 + cf * 2;
    {
        const int e = i32 >> 4, d = i32 & 15;
#pragma unroll
        for (int sub = 0; sub < 2; ++sub)
            st16(sws, WS_VF + (((h0 + e) * 64 + tt) * 2 + sub) * 512 + hh * 256 + d * 16,
                 u32x4{cvtpk(av[8 * sub + 0], av[8 * sub + 1]), cvtpk(av[8 * sub + 2], av[8 * sub + 3]), cvtpk(av[8 * sub + 4], av[8 * sub + 5]), cvtpk(av[8 * sub + 6], av[8 * sub + 7])});
    }
    {
        const int pi = (i32 & 19) | ((i32 & 4) << 1) | ((i32 & 8) >> 1);
#pragma unroll
        for (int e = 0; e < 2; ++e) {
            float q8[8], k8[8]; float sq = 0.f, sk = 0.f;
#pragma unroll
            for (int j = 0; j < 8; ++j) { q8[j] = aq[8 * e + j] * QSCALE; k8[j] = ak[8 * e + j]; sq += q8[j] * q8[j]; sk += k8[j] * k8[j]; }
            st16(sws, WS_KF + (((h0 + e) * 64 + tt) * 64 + pi + 32 * hh) * 16, u32x4{cvtpk(k8[0], k8[1]), cvtpk(k8[2], k8[3]), cvtpk(k8[4], k8[5]), cvtpk(k8[6], k8[7])});
            st16(sws, WS_QF + (((h0 + e) * 64 + tt) * 64 + i32 + 32 * hh) * 16, u32x4{cvtpk(q8[0], q8[1]), cvtpk(q8[2], q8[3]), cvtpk(q8[4], q8[5]), cvtpk(q8[6], q8[7])});
            sq += swap32(sq); sk += swap32(sk);
            sq = dppmax<0x142, 0xa>(rowmax16(sq)); sk = dppmax<0x142, 0xa>(rowmax16(sk));
            if (lane == 31) { LAS float* nr = (LAS float*)(lds + NRM_LDS) + (cf * 2 + tl) * 4; nr[e] = sk; nr[2 + e] = sq; }
        }
    }
    STAMP(0, 10);
    if (FUSED) WAITV(0);
    WAITL0();
    if (lds_inc(lds, CNT_QKV) == 3 && lane < 32) {
        const int hl = lane >> 3;
        LAS const float* nr = (LAS const float*)(lds + NRM_LDS);
        const float vk = __builtin_fmaxf(nr[((hl >> 1) * 2 + 0) * 4 + (hl & 1)], nr[((hl >> 1) * 2 + 1) * 4 + (hl & 1)]);
        const float vq = __builtin_fmaxf(nr[((hl >> 1) * 2 + 0) * 4 + 2 + (hl & 1)], nr[((hl >> 1) * 2 + 1) * 4 + 2 + (hl & 1)]);
        put_flag(ws, WS_F1A + (((g4 * 4 + hl) * 8 + (lane & 7)) * 32 + rb) * 8, (tag << 24) | (code12(vk) << 12) | code12(vq));
        STAMP(0, 11);
    }
}

#define ATTN_LOOP_ASM \
  "buffer_load_dwordx4 v[120:123], %[qo], %[srd], 0 offen" SC1 "\n" \
  "buffer_load_dwordx4 v[124:127], %[qo], %[srd], 0 offen offset:1024" SC1 "\n" \
  "s_add_u32 m0, %[l0], 0\n" \
  "s_add_u32 %[t], %[kb], 0\n" \
  "buffer_load_dwordx4 %[vo], %[srd], %[t] offen" SC1 " lds\n" \
  "s_add_u32 m0, %[l0], 65536\n" \
  "s_add_u32 %[t], %[kb], 2097152\n" \
  "buffer_load_dwordx4 %[vo], %[srd], %[t] offen" SC1 " lds\n" \
  "s_add_u32 m0, %[l0], 32768\n" \
  "s_add_u32 %[t], %[kb], 32768\n" \
  "buffer_load_dwordx4 %[vo], %[srd], %[t] offen" SC1 " lds\n" \
  "s_add_u32 m0, %[l0], 98304\n" \
  "s_add_u32 %[t], %[kb], 2129920\n" \
  "buffer_load_dwordx4 %[vo], %[srd], %[t] offen" SC1 " lds\n" \
  "s_add_u32 m0, %[l0], 8192\n" \
  "s_add_u32 %[t], %[kb], 8192\n" \
  "buffer_load_dwordx4 %[vo], %[srd], %[t] offen" SC1 " lds\n" \
  "s_add_u32 m0, %[l0], 73728\n" \
  "s_add_u32 %[t], %[kb], 2105344\n" \
  "buffer_load_dwordx4 %[vo], %[srd], %[t] offen" SC1 " lds\n" \
  "s_add_u32 m0, %[l0], 40960\n" \
  "s_add_u32 %[t], %[kb], 40960\n" \
  "buffer_load_dwordx4 %[vo], %[srd], %[t] offen" SC1 " lds\n" \
  "s_add_u32 m0, %[l0], 106496\n" \
  "s_add_u32 %[t], %[kb], 2138112\n" \
  "buffer_load_dwordx4 %[vo], %[srd], %[t] offen" SC1 " lds\n" \
  "s_add_u32 m0, %[l0], 16384\n" \
  "s_add_u32 %[t], %[kb], 16384\n" \
  "buffer_load_dwordx4 %[vo], %[srd], %[t] offen" SC1 " lds\n" \
  "s_add_u32 m0, %[l0], 81920\n" \
  "s_add_u32 %[t], %[kb], 2113536\n" \
  "buffer_load_dwordx4 %[vo], %[srd], %[t] offen" SC1 " lds\n" \
  "s_add_u32 m0, %[l0], 49152\n" \
  "s_add_u32 %[t], %[kb], 49152\n" \
  "buffer_load_dwordx4 %[vo], %[srd], %[t] offen" SC1 " lds\n" \
  "s_add_u32 m0, %[l0], 114688\n" \
  "s_add_u32 %[t], %[kb], 2146304\n" \
  "buffer_load_dwordx4 %[vo], %[srd], %[t] offen" SC1 " lds\n" \
  "s_add_u32 m0, %[l0], 24576\n" \
  "s_add_u32 %[t], %[kb], 24576\n" \
  "buffer_load_dwordx4 %[vo], %[srd], %[t] offen" SC1 " lds\n" \
  "s_add_u32 m0, %[l0], 90112\n" \
  "s_add_u32 %[t], %[kb], 2121728\n" \
  "buffer_load_dwordx4 %[vo], %[srd], %[t] offen" SC1 " lds\n" \
  "s_add_u32 m0, %[l0], 57344\n" \
  "s_add_u32 %[t], %[kb], 57344\n" \
  "buffer_load_dwordx4 %[vo], %[srd], %[t] offen" SC1 " lds\n" \
  "s_add_u32 m0, %[l0], 122880\n" \
  "s_add_u32 %[t], %[kb], 2154496\n" \
  "buffer_load_dwordx4 %[vo], %[srd], %[t] offen" SC1 " lds\n" \
  "v_mov_b32 v117, 1\n" \
  "s_waitcnt vmcnt(%[w0])\n" \
  "s_mov_b64 exec, 1\n" \
  "ds_add_u32 %[cnt], v117\n" \
  "s_mov_b64 exec, -1\n" \
  "Sa%=:\n" \
  "ds_read_b32 v118, %[cnt]\n" \
  "s_waitcnt lgkmcnt(0)\n" \
  "v_readfirstlane_b32 %[t], v118\n" \
  "s_nop 3\n" \
  "s_cmp_ge_u32 %[t], 8\n" \
  "s_cbranch_scc1 Da%=\n" \
  "s_sleep 1\n" \
  "s_branch Sa%=\n" \
  "Da%=:\n" \
  "v_mov_b32 v116, 0xdddd\n" \
  "ds_read_b128 v[96:99], %[ka] offset:0\n" \
  "v_mov_b32 v100, %[fill]\n" \
  "v_mov_b32 v101, %[fill]\n" \
  "v_mov_b32 v102, %[fill]\n" \
  "v_mov_b32 v103, %[fill]\n" \
  "v_mov_b32 v104, %[fill]\n" \
  "v_mov_b32 v105, %[fill]\n" \
  "v_mov_b32 v106, %[fill]\n" \
  "v_mov_b32 v107, %[fill]\n" \
  "v_mov_b32 v108, %[fill]\n" \
  "v_mov_b32 v109, %[fill]\n" \
  "v_mov_b32 v110, %[fill]\n" \
  "v_mov_b32 v111, %[fill]\n" \
  "v_mov_b32 v112, %[fill]\n" \
  "v_mov_b32 v113, %[fill]\n" \
  "v_mov_b32 v114, %[fill]\n" \
  "v_mov_b32 v115, %[fill]\n" \
  "s_mov_b64 exec, %[vm]\n" \
  "ds_read_b128 v[100:103], %[va] offset:0\n" \
  "ds_read_b128 v[104:107], %[va] offset:512\n" \
  "s_mov_b64 exec, -1\n" \
  "s_waitcnt lgkmcnt(2)\n" \
  "v_mfma_f32_32x32x16_bf16 v[64:79], v[96:99], v[120:123], 0\n" \
  "v_mfma_f32_32x32x16_bf16 v[80:95], v[96:99], v[124:127], 0\n" \
  "s_nop 7\n" \
  "s_nop 3\n" \
  "v_exp_f32 v64, v64\n" \
  "v_exp_f32 v65, v65\n" \
  "v_exp_f32 v66, v66\n" \
  "v_exp_f32 v67, v67\n" \
  "v_exp_f32 v68, v68\n" \
  "s_mov_b32 %[g], 0\n" \
  "LOOP%=:\n" \
  "s_cmp_eq_u32 %[g], 1\n" \
  "s_cbranch_scc1 W1%=\n" \
  "s_cmp_eq_u32 %[g], 3\n" \
  "s_cbranch_scc1 W3%=\n" \
  "BODY%=:\n" \
  "s_mov_b64 exec, %[vm]\n" \
  "ds_read_b128 v[108:111], %[va] offset:1024\n" \
  "ds_read_b128 v[112:115], %[va] offset:1536\n" \
  "s_mov_b64 exec, -1\n" \
  "ds_read_b128 v[96:99], %[ka] offset:1024\n" \
  "v_exp_f32 v69, v69\n" \
  "v_exp_f32 v70, v70\n" \
  "v_exp_f32 v71, v71\n" \
  "v_exp_f32 v72, v72\n" \
  "v_exp_f32 v73, v73\n" \
  "s_waitcnt lgkmcnt(3)\n" \
  "v_smfmac_f32_32x32x32_bf16 %[o1], v[100:103], v[64:71], v116\n" \
  "v_exp_f32 v74, v74\n" \
  "v_exp_f32 v75, v75\n" \
  "v_exp_f32 v76, v76\n" \
  "v_exp_f32 v77, v77\n" \
  "v_exp_f32 v78, v78\n" \
  "v_exp_f32 v79, v79\n" \
  "v_exp_f32 v80, v80\n" \
  "v_exp_f32 v81, v81\n" \
  "v_smfmac_f32_32x32x32_bf16 %[o1], v[104:107], v[72:79], v116\n" \
  "v_exp_f32 v82, v82\n" \
  "v_exp_f32 v83, v83\n" \
  "v_exp_f32 v84, v84\n" \
  "s_waitcnt lgkmcnt(0)\n" \
  "v_mfma_f32_32x32x16_bf16 v[64:79], v[96:99], v[120:123], 0\n" \
  "v_exp_f32 v85, v85\n" \
  "v_exp_f32 v86, v86\n" \
  "v_exp_f32 v87, v87\n" \
  "v_exp_f32 v88, v88\n" \
  "v_exp_f32 v89, v89\n" \
  "v_smfmac_f32_32x32x32_bf16 %[o2], v[100:103], v[80:87], v116\n" \
  "v_exp_f32 v90, v90\n" \
  "v_exp_f32 v91, v91\n" \
  "v_exp_f32 v92, v92\n" \
  "v_exp_f32 v93, v93\n" \
  "v_exp_f32 v94, v94\n" \
  "v_exp_f32 v95, v95\n" \
  "v_exp_f32 v64, v64\n" \
  "v_exp_f32 v65, v65\n" \
  "v_smfmac_f32_32x32x32_bf16 %[o2], v[104:107], v[88:95], v116\n" \
  "v_exp_f32 v66, v66\n" \
  "v_exp_f32 v67, v67\n" \
  "v_exp_f32 v68, v68\n" \
  "v_mfma_f32_32x32x16_bf16 v[80:95], v[96:99], v[124:127], 0\n" \
  "s_mov_b64 exec, %[vm]\n" \
  "ds_read_b128 v[100:103], %[va] offset:2048\n" \
  "ds_read_b128 v[104:107], %[va] offset:2560\n" \
  "s_mov_b64 exec, -1\n" \
  "ds_read_b128 v[96:99], %[ka] offset:2048\n" \
  "v_exp_f32 v69, v69\n" \
  "v_exp_f32 v70, v70\n" \
  "v_exp_f32 v71, v71\n" \
  "v_exp_f32 v72, v72\n" \
  "v_exp_f32 v73, v73\n" \
  "s_waitcnt lgkmcnt(3)\n" \
  "v_smfmac_f32_32x32x32_bf16 %[o1], v[108:111], v[64:71], v116\n" \
  "v_exp_f32 v74, v74\n" \
  "v_exp_f32 v75, v75\n" \
  "v_exp_f32 v76, v76\n" \
  "v_exp_f32 v77, v77\n" \
  "v_exp_f32 v78, v78\n" \
  "v_exp_f32 v79, v79\n" \
  "v_exp_f32 v80, v80\n" \
  "v_exp_f32 v81, v81\n" \
  "v_smfmac_f32_32x32x32_bf16 %[o1], v[112:115], v[72:79], v116\n" \
  "v_exp_f32 v82, v82\n" \
  "v_exp_f32 v83, v83\n" \
  "v_exp_f32 v84, v84\n" \
  "s_waitcnt lgkmcnt(0)\n" \
  "v_mfma_f32_32x32x16_bf16 v[64:79], v[96:99], v[120:123], 0\n" \
  "v_exp_f32 v85, v85\n" \
  "v_exp_f32 v86, v86\n" \
  "v_exp_f32 v87, v87\n" \
  "v_exp_f32 v88, v88\n" \
  "v_exp_f32 v89, v89\n" \
  "v_smfmac_f32_32x32x32_bf16 %[o2], v[108:111], v[80:87], v116\n" \
  "v_exp_f32 v90, v90\n" \
  "v_exp_f32 v91, v91\n" \
  "v_exp_f32 v92, v92\n" \
  "v_exp_f32 v93, v93\n" \
  "v_exp_f32 v94, v94\n" \
  "v_exp_f32 v95, v95\n" \
  "v_exp_f32 v64, v64\n" \
  "v_exp_f32 v65, v65\n" \
  "v_smfmac_f32_32x32x32_bf16 %[o2], v[112:115], v[88:95], v116\n" \
  "v_exp_f32 v66, v66\n" \
  "v_exp_f32 v67, v67\n" \
  "v_exp_f32 v68, v68\n" \
  "v_mfma_f32_32x32x16_bf16 v[80:95], v[96:99], v[124:127], 0\n" \
  "s_mov_b64 exec, %[vm]\n" \
  "ds_read_b128 v[108:111], %[va] offset:3072\n" \
  "ds_read_b128 v[112:115], %[va] offset:3584\n" \
  "s_mov_b64 exec, -1\n" \
  "ds_read_b128 v[96:99], %[ka] offset:3072\n" \
  "v_exp_f32 v69, v69\n" \
  "v_exp_f32 v70, v70\n" \
  "v_exp_f32 v71, v71\n" \
  "v_exp_f32 v72, v72\n" \
  "v_exp_f32 v73, v73\n" \
  "s_waitcnt lgkmcnt(3)\n" \
  "v_smfmac_f32_32x32x32_bf16 %[o1], v[100:103], v[64:71], v116\n" \
  "v_exp_f32 v74, v74\n" \
  "v_exp_f32 v75, v75\n" \
  "v_exp_f32 v76, v76\n" \
  "v_exp_f32 v77, v77\n" \
  "v_exp_f32 v78, v78\n" \
  "v_exp_f32 v79, v79\n" \
  "v_exp_f32 v80, v80\n" \
  "v_exp_f32 v81, v81\n" \
  "v_smfmac_f32_32x32x32_bf16 %[o1], v[104:107], v[72:79], v116\n" \
  "v_exp_f32 v82, v82\n" \
  "v_exp_f32 v83, v83\n" \
  "v_exp_f32 v84, v84\n" \
  "s_waitcnt lgkmcnt(0)\n" \
  "v_mfma_f32_32x32x16_bf16 v[64:79], v[96:99], v[120:123], 0\n" \
  "v_exp_f32 v85, v85\n" \
  "v_exp_f32 v86, v86\n" \
  "v_exp_f32 v87, v87\n" \
  "v_exp_f32 v88, v88\n" \
  "v_exp_f32 v89, v89\n" \
  "v_smfmac_f32_32x32x32_bf16 %[o2], v[100:103], v[80:87], v116\n" \
  "v_exp_f32 v90, v90\n" \
  "v_exp_f32 v91, v91\n" \
  "v_exp_f32 v92, v92\n" \
  "v_exp_f32 v93, v93\n" \
  "v_exp_f32 v94, v94\n" \
  "v_exp_f32 v95, v95\n" \
  "v_exp_f32 v64, v64\n" \
  "v_exp_f32 v65, v65\n" \
  "v_smfmac_f32_32x32x32_bf16 %[o2], v[104:107], v[88:95], v116\n" \
  "v_exp_f32 v66, v66\n" \
  "v_exp_f32 v67, v67\n" \
  "v_exp_f32 v68, v68\n" \
  "v_mfma_f32_32x32x16_bf16 v[80:95], v[96:99], v[124:127], 0\n" \
  "s_mov_b64 exec, %[vm]\n" \
  "ds_read_b128 v[100:103], %[va] offset:4096\n" \
  "ds_read_b128 v[104:107], %[va] offset:4608\n" \
  "s_mov_b64 exec, -1\n" \
  "ds_read_b128 v[96:99], %[ka] offset:4096\n" \
  "v_exp_f32 v69, v69\n" \
  "v_exp_f32 v70, v70\n" \
  "v_exp_f32 v71, v71\n" \
  "v_exp_f32 v72, v72\n" \
  "v_exp_f32 v73, v73\n" \
  "s_waitcnt lgkmcnt(3)\n" \
  "v_smfmac_f32_32x32x32_bf16 %[o1], v[108:111], v[64:71], v116\n" \
  "v_exp_f32 v74, v74\n" \
  "v_exp_f32 v75, v75\n" \
  "v_exp_f32 v76, v76\n" \
  "v_exp_f32 v77, v77\n" \
  "v_exp_f32 v78, v78\n" \
  "v_exp_f32 v79, v79\n" \
  "v_exp_f32 v80, v80\n" \
  "v_exp_f32 v81, v81\n" \
  "v_smfmac_f32_32x32x32_bf16 %[o1], v[112:115], v[72:79], v116\n" \
  "v_exp_f32 v82, v82\n" \
  "v_exp_f32 v83, v83\n" \
  "v_exp_f32 v84, v84\n" \
  "s_waitcnt lgkmcnt(0)\n" \
  "v_mfma_f32_32x32x16_bf16 v[64:79], v[96:99], v[120:123], 0\n" \
  "v_exp_f32 v85, v85\n" \
  "v_exp_f32 v86, v86\n" \
  "v_exp_f32 v87, v87\n" \
  "v_exp_f32 v88, v88\n" \
  "v_exp_f32 v89, v89\n" \
  "v_smfmac_f32_32x32x32_bf16 %[o2], v[108:111], v[80:87], v116\n" \
  "v_exp_f32 v90, v90\n" \
  "v_exp_f32 v91, v91\n" \
  "v_exp_f32 v92, v92\n" \
  "v_exp_f32 v93, v93\n" \
  "v_exp_f32 v94, v94\n" \
  "v_exp_f32 v95, v95\n" \
  "v_exp_f32 v64, v64\n" \
  "v_exp_f32 v65, v65\n" \
  "v_smfmac_f32_32x32x32_bf16 %[o2], v[112:115], v[88:95], v116\n" \
  "v_exp_f32 v66, v66\n" \
  "v_exp_f32 v67, v67\n" \
  "v_exp_f32 v68, v68\n" \
  "v_mfma_f32_32x32x16_bf16 v[80:95], v[96:99], v[124:127], 0\n" \
  "v_add_u32 %[ka], 4096, %[ka]\n" \
  "v_add_u32 %[va], 4096, %[va]\n" \
  "s_add_u32 %[g], %[g], 1\n" \
  "s_cmp_lt_u32 %[g], 8\n" \
  "s_cbranch_scc1 LOOP%=\n" \
  "s_branch END%=\n" \
  "W1%=:\n" \
  "s_waitcnt vmcnt(%[w1])\n" \
  "s_mov_b64 exec, 1\n" \
  "ds_add_u32 %[cnt], v117\n" \
  "s_mov_b64 exec, -1\n" \
  "Sb%=:\n" \
  "ds_read_b32 v118, %[cnt]\n" \
  "s_waitcnt lgkmcnt(0)\n" \
  "v_readfirstlane_b32 %[t], v118\n" \
  "s_nop 3\n" \
  "s_cmp_ge_u32 %[t], 16\n" \
  "s_cbranch_scc1 Db%=\n" \
  "s_sleep 1\n" \
  "s_branch Sb%=\n" \
  "Db%=:\n" \
  "s_branch BODY%=\n" \
  "W3%=:\n" \
  "s_waitcnt vmcnt(0)\n" \
  "s_mov_b64 exec, 1\n" \
  "ds_add_u32 %[cnt], v117\n" \
  "s_mov_b64 exec, -1\n" \
  "Sc%=:\n" \
  "ds_read_b32 v118, %[cnt]\n" \
  "s_waitcnt lgkmcnt(0)\n" \
  "v_readfirstlane_b32 %[t], v118\n" \
  "s_nop 3\n" \
  "s_cmp_ge_u32 %[t], 24\n" \
  "s_cbranch_scc1 Dc%=\n" \
  "s_sleep 1\n" \
  "s_branch Sc%=\n" \
  "Dc%=:\n" \
  "s_branch BODY%=\n" \
  "END%=:\n" \
  "s_waitcnt lgkmcnt(0)\n" \
  "s_nop 15\n" \
  "s_nop 7\n"
#define ATTN_LOOP_CLOBBERS "v64", "v65", "v66", "v67", "v68", "v69", "v70", "v71", "v72", "v73", "v74", "v75", "v76", "v77", "v78", "v79", "v80", "v81", "v82", "v83", "v84", "v85", "v86", "v87", "v88", "v89", "v90", "v91", "v92", "v93", "v94", "v95", "v96", "v97", "v98", "v99", "v100", "v101", "v102", "v103", "v104", "v105", "v106", "v107", "v108", "v109", "v110", "v111", "v112", "v113", "v114", "v115", "v116", "v117", "v118", "v119", "v120", "v121", "v122", "v123", "v124", "v125", "v126", "v127"
#define CONV_LOOP_ASM \
  "ds_read2_b32 v[64:65], %[ap] offset0:0 offset1:1\n" \
  "ds_read2_b32 v[66:67], %[ap] offset0:2 offset1:3\n" \
  "ds_read_b128 v[76:79], %[bp] offset:0\n" \
  "ds_read_b128 v[72:75], %[bp] offset:2560\n" \
  "ds_read2_b32 v[68:69], %[aq] offset0:0 offset1:1\n" \
  "ds_read2_b32 v[70:71], %[aq] offset0:2 offset1:3\n" \
  "ds_read2_b32 v[80:81], %[ap] offset0:8 offset1:9\n" \
  "ds_read2_b32 v[82:83], %[ap] offset0:10 offset1:11\n" \
  "s_waitcnt lgkmcnt(2)\n" \
  "v_mfma_f32_32x32x16_bf16 %[a1], v[64:67], v[76:79], %[a1]\n" \
  "ds_read_b128 v[92:95], %[bp] offset:32\n" \
  "ds_read_b128 v[88:91], %[bp] offset:2592\n" \
  "v_mfma_f32_32x32x16_bf16 %[a0], v[64:67], v[72:75], %[a0]\n" \
  "ds_read2_b32 v[84:85], %[aq] offset0:8 offset1:9\n" \
  "ds_read2_b32 v[86:87], %[aq] offset0:10 offset1:11\n" \
  "v_mfma_f32_32x32x16_bf16 %[a1], v[68:71], v[72:75], %[a1]\n" \
  "ds_read2_b32 v[64:65], %[ap] offset0:16 offset1:17\n" \
  "ds_read2_b32 v[66:67], %[ap] offset0:18 offset1:19\n" \
  "s_waitcnt lgkmcnt(2)\n" \
  "v_mfma_f32_32x32x16_bf16 %[a1], v[80:83], v[92:95], %[a1]\n" \
  "ds_read_b128 v[76:79], %[bp] offset:80\n" \
  "ds_read_b128 v[72:75], %[bp] offset:2640\n" \
  "v_mfma_f32_32x32x16_bf16 %[a0], v[80:83], v[88:91], %[a0]\n" \
  "ds_read2_b32 v[68:69], %[aq] offset0:16 offset1:17\n" \
  "ds_read2_b32 v[70:71], %[aq] offset0:18 offset1:19\n" \
  "v_mfma_f32_32x32x16_bf16 %[a1], v[84:87], v[88:91], %[a1]\n" \
  "ds_read2_b32 v[80:81], %[ap] offset0:24 offset1:25\n" \
  "ds_read2_b32 v[82:83], %[ap] offset0:26 offset1:27\n" \
  "s_waitcnt lgkmcnt(2)\n" \
  "v_mfma_f32_32x32x16_bf16 %[a1], v[64:67], v[76:79], %[a1]\n" \
  "ds_read_b128 v[92:95], %[bp] offset:112\n" \
  "ds_read_b128 v[88:91], %[bp] offset:2672\n" \
  "v_mfma_f32_32x32x16_bf16 %[a0], v[64:67], v[72:75], %[a0]\n" \
  "ds_read2_b32 v[84:85], %[aq] offset0:24 offset1:25\n" \
  "ds_read2_b32 v[86:87], %[aq] offset0:26 offset1:27\n" \
  "v_mfma_f32_32x32x16_bf16 %[a1], v[68:71], v[72:75], %[a1]\n" \
  "ds_read2_b32 v[64:65], %[ap] offset0:32 offset1:33\n" \
  "ds_read2_b32 v[66:67], %[ap] offset0:34 offset1:35\n" \
  "s_waitcnt lgkmcnt(2)\n" \
  "v_mfma_f32_32x32x16_bf16 %[a1], v[80:83], v[92:95], %[a1]\n" \
  "ds_read_b128 v[76:79], %[bp] offset:160\n" \
  "ds_read_b128 v[72:75], %[bp] offset:2720\n" \
  "v_mfma_f32_32x32x16_bf16 %[a0], v[80:83], v[88:91], %[a0]\n" \
  "ds_read2_b32 v[68:69], %[aq] offset0:32 offset1:33\n" \
  "ds_read2_b32 v[70:71], %[aq] offset0:34 offset1:35\n" \
  "v_mfma_f32_32x32x16_bf16 %[a1], v[84:87], v[88:91], %[a1]\n" \
  "ds_read2_b32 v[80:81], %[ap] offset0:40 offset1:41\n" \
  "ds_read2_b32 v[82:83], %[ap] offset0:42 offset1:43\n" \
  "s_waitcnt lgkmcnt(2)\n" \
  "v_mfma_f32_32x32x16_bf16 %[a1], v[64:67], v[76:79], %[a1]\n" \
  "ds_read_b128 v[92:95], %[bp] offset:192\n" \
  "ds_read_b128 v[88:91], %[bp] offset:2752\n" \
  "v_mfma_f32_32x32x16_bf16 %[a0], v[64:67], v[72:75], %[a0]\n" \
  "ds_read2_b32 v[84:85], %[aq] offset0:40 offset1:41\n" \
  "ds_read2_b32 v[86:87], %[aq] offset0:42 offset1:43\n" \
  "v_mfma_f32_32x32x16_bf16 %[a1], v[68:71], v[72:75], %[a1]\n" \
  "ds_read2_b32 v[64:65], %[ap] offset0:48 offset1:49\n" \
  "ds_read2_b32 v[66:67], %[ap] offset0:50 offset1:51\n" \
  "s_waitcnt lgkmcnt(2)\n" \
  "v_mfma_f32_32x32x16_bf16 %[a1], v[80:83], v[92:95], %[a1]\n" \
  "ds_read_b128 v[76:79], %[bp] offset:240\n" \
  "ds_read_b128 v[72:75], %[bp] offset:2800\n" \
  "v_mfma_f32_32x32x16_bf16 %[a0], v[80:83], v[88:91], %[a0]\n" \
  "ds_read2_b32 v[68:69], %[aq] offset0:48 offset1:49\n" \
  "ds_read2_b32 v[70:71], %[aq] offset0:50 offset1:51\n" \
  "v_mfma_f32_32x32x16_bf16 %[a1], v[84:87], v[88:91], %[a1]\n" \
  "ds_read2_b32 v[80:81], %[ap] offset0:56 offset1:57\n" \
  "ds_read2_b32 v[82:83], %[ap] offset0:58 offset1:59\n" \
  "s_waitcnt lgkmcnt(2)\n" \
  "v_mfma_f32_32x32x16_bf16 %[a1], v[64:67], v[76:79], %[a1]\n" \
  "ds_read_b128 v[92:95], %[bp] offset:272\n" \
  "ds_read_b128 v[88:91], %[bp] offset:2832\n" \
  "v_mfma_f32_32x32x16_bf16 %[a0], v[64:67], v[72:75], %[a0]\n" \
  "ds_read2_b32 v[84:85], %[aq] offset0:56 offset1:57\n" \
  "ds_read2_b32 v[86:87], %[aq] offset0:58 offset1:59\n" \
  "v_mfma_f32_32x32x16_bf16 %[a1], v[68:71], v[72:75], %[a1]\n" \
  "ds_read2_b32 v[64:65], %[ap] offset0:64 offset1:65\n" \
  "ds_read2_b32 v[66:67], %[ap] offset0:66 offset1:67\n" \
  "s_waitcnt lgkmcnt(2)\n" \
  "v_mfma_f32_32x32x16_bf16 %[a1], v[80:83], v[92:95], %[a1]\n" \
  "ds_read_b128 v[76:79], %[bp] offset:320\n" \
  "ds_read_b128 v[72:75], %[bp] offset:2880\n" \
  "v_mfma_f32_32x32x16_bf16 %[a0], v[80:83], v[88:91], %[a0]\n" \
  "ds_read2_b32 v[68:69], %[aq] offset0:64 offset1:65\n" \
  "ds_read2_b32 v[70:71], %[aq] offset0:66 offset1:67\n" \
  "v_mfma_f32_32x32x16_bf16 %[a1], v[84:87], v[88:91], %[a1]\n" \
  "ds_read2_b32 v[80:81], %[ap] offset0:72 offset1:73\n" \
  "ds_read2_b32 v[82:83], %[ap] offset0:74 offset1:75\n" \
  "s_waitcnt lgkmcnt(2)\n" \
  "v_mfma_f32_32x32x16_bf16 %[a1], v[64:67], v[76:79], %[a1]\n" \
  "ds_read_b128 v[92:95], %[bp] offset:352\n" \
  "ds_read_b128 v[88:91], %[bp] offset:2912\n" \
  "v_mfma_f32_32x32x16_bf16 %[a0], v[64:67], v[72:75], %[a0]\n" \
  "ds_read2_b32 v[84:85], %[aq] offset0:72 offset1:73\n" \
  "ds_read2_b32 v[86:87], %[aq] offset0:74 offset1:75\n" \
  "v_mfma_f32_32x32x16_bf16 %[a1], v[68:71], v[72:75], %[a1]\n" \
  "ds_read2_b32 v[64:65], %[ap] offset0:80 offset1:81\n" \
  "ds_read2_b32 v[66:67], %[ap] offset0:82 offset1:83\n" \
  "s_waitcnt lgkmcnt(2)\n" \
  "v_mfma_f32_32x32x16_bf16 %[a1], v[80:83], v[92:95], %[a1]\n" \
  "ds_read_b128 v[76:79], %[bp] offset:400\n" \
  "ds_read_b128 v[72:75], %[bp] offset:2960\n" \
  "v_mfma_f32_32x32x16_bf16 %[a0], v[80:83], v[88:91], %[a0]\n" \
  "ds_read2_b32 v[68:69], %[aq] offset0:80 offset1:81\n" \
  "ds_read2_b32 v[70:71], %[aq] offset0:82 offset1:83\n" \
  "v_mfma_f32_32x32x16_bf16 %[a1], v[84:87], v[88:91], %[a1]\n" \
  "ds_read2_b32 v[80:81], %[ap] offset0:88 offset1:89\n" \
  "ds_read2_b32 v[82:83], %[ap] offset0:90 offset1:91\n" \
  "s_waitcnt lgkmcnt(2)\n" \
  "v_mfma_f32_32x32x16_bf16 %[a1], v[64:67], v[76:79], %[a1]\n" \
  "ds_read_b128 v[92:95], %[bp] offset:432\n" \
  "ds_read_b128 v[88:91], %[bp] offset:2992\n" \
  "v_mfma_f32_32x32x16_bf16 %[a0], v[64:67], v[72:75], %[a0]\n" \
  "ds_read2_b32 v[84:85], %[aq] offset0:88 offset1:89\n" \
  "ds_read2_b32 v[86:87], %[aq] offset0:90 offset1:91\n" \
  "v_mfma_f32_32x32x16_bf16 %[a1], v[68:71], v[72:75], %[a1]\n" \
  "ds_read2_b32 v[64:65], %[ap] offset0:96 offset1:97\n" \
  "ds_read2_b32 v[66:67], %[ap] offset0:98 offset1:99\n" \
  "s_waitcnt lgkmcnt(2)\n" \
  "v_mfma_f32_32x32x16_bf16 %[a1], v[80:83], v[92:95], %[a1]\n" \
  "ds_read_b128 v[76:79], %[bp] offset:480\n" \
  "ds_read_b128 v[72:75], %[bp] offset:3040\n" \
  "v_mfma_f32_32x32x16_bf16 %[a0], v[80:83], v[88:91], %[a0]\n" \
  "ds_read2_b32 v[68:69], %[aq] offset0:96 offset1:97\n" \
  "ds_read2_b32 v[70:71], %[aq] offset0:98 offset1:99\n" \
  "v_mfma_f32_32x32x16_bf16 %[a1], v[84:87], v[88:91], %[a1]\n" \
  "ds_read2_b32 v[80:81], %[ap] offset0:104 offset1:105\n" \
  "ds_read2_b32 v[82:83], %[ap] offset0:106 offset1:107\n" \
  "s_waitcnt lgkmcnt(2)\n" \
  "v_mfma_f32_32x32x16_bf16 %[a1], v[64:67], v[76:79], %[a1]\n" \
  "ds_read_b128 v[92:95], %[bp] offset:512\n" \
  "ds_read_b128 v[88:91], %[bp] offset:3072\n" \
  "v_mfma_f32_32x32x16_bf16 %[a0], v[64:67], v[72:75], %[a0]\n" \
  "ds_read2_b32 v[84:85], %[aq] offset0:104 offset1:105\n" \
  "ds_read2_b32 v[86:87], %[aq] offset0:106 offset1:107\n" \
  "v_mfma_f32_32x32x16_bf16 %[a1], v[68:71], v[72:75], %[a1]\n" \
  "ds_read2_b32 v[64:65], %[ap] offset0:112 offset1:113\n" \
  "ds_read2_b32 v[66:67], %[ap] offset0:114 offset1:115\n" \
  "s_waitcnt lgkmcnt(2)\n" \
  "v_mfma_f32_32x32x16_bf16 %[a1], v[80:83], v[92:95], %[a1]\n" \
  "ds_read_b128 v[76:79], %[bp] offset:560\n" \
  "ds_read_b128 v[72:75], %[bp] offset:3120\n" \
  "v_mfma_f32_32x32x16_bf16 %[a0], v[80:83], v[88:91], %[a0]\n" \
  "ds_read2_b32 v[68:69], %[aq] offset0:112 offset1:113\n" \
  "ds_read2_b32 v[70:71], %[aq] offset0:114 offset1:115\n" \
  "v_mfma_f32_32x32x16_bf16 %[a1], v[84:87], v[88:91], %[a1]\n" \
  "ds_read2_b32 v[80:81], %[ap] offset0:120 offset1:121\n" \
  "ds_read2_b32 v[82:83], %[ap] offset0:122 offset1:123\n" \
  "s_waitcnt lgkmcnt(2)\n" \
  "v_mfma_f32_32x32x16_bf16 %[a1], v[64:67], v[76:79], %[a1]\n" \
  "ds_read_b128 v[92:95], %[bp] offset:592\n" \
  "ds_read_b128 v[88:91], %[bp] offset:3152\n" \
  "v_mfma_f32_32x32x16_bf16 %[a0], v[64:67], v[72:75], %[a0]\n" \
  "ds_read2_b32 v[84:85], %[aq] offset0:120 offset1:121\n" \
  "ds_read2_b32 v[86:87], %[aq] offset0:122 offset1:123\n" \
  "v_mfma_f32_32x32x16_bf16 %[a1], v[68:71], v[72:75], %[a1]\n" \
  "ds_read2_b32 v[64:65], %[ap] offset0:128 offset1:129\n" \
  "ds_read2_b32 v[66:67], %[ap] offset0:130 offset1:131\n" \
  "s_waitcnt lgkmcnt(2)\n" \
  "v_mfma_f32_32x32x16_bf16 %[a1], v[80:83], v[92:95], %[a1]\n" \
  "ds_read_b128 v[76:79], %[bp] offset:640\n" \
  "ds_read_b128 v[72:75], %[bp] offset:3200\n" \
  "v_mfma_f32_32x32x16_bf16 %[a0], v[80:83], v[88:91], %[a0]\n" \
  "ds_read2_b32 v[68:69], %[aq] offset0:128 offset1:129\n" \
  "ds_read2_b32 v[70:71], %[aq] offset0:130 offset1:131\n" \
  "v_mfma_f32_32x32x16_bf16 %[a1], v[84:87], v[88:91], %[a1]\n" \
  "ds_read2_b32 v[80:81], %[ap] offset0:136 offset1:137\n" \
  "ds_read2_b32 v[82:83], %[ap] offset0:138 offset1:139\n" \
  "s_waitcnt lgkmcnt(2)\n" \
  "v_mfma_f32_32x32x16_bf16 %[a1], v[64:67], v[76:79], %[a1]\n" \
  "ds_read_b128 v[92:95], %[bp] offset:672\n" \
  "ds_read_b128 v[88:91], %[bp] offset:3232\n" \
  "v_mfma_f32_32x32x16_bf16 %[a0], v[64:67], v[72:75], %[a0]\n" \
  "ds_read2_b32 v[84:85], %[aq] offset0:136 offset1:137\n" \
  "ds_read2_b32 v[86:87], %[aq] offset0:138 offset1:139\n" \
  "v_mfma_f32_32x32x16_bf16 %[a1], v[68:71], v[72:75], %[a1]\n" \
  "ds_read2_b32 v[64:65], %[ap] offset0:144 offset1:145\n" \
  "ds_read2_b32 v[66:67], %[ap] offset0:146 offset1:147\n" \
  "s_waitcnt lgkmcnt(2)\n" \
  "v_mfma_f32_32x32x16_bf16 %[a1], v[80:83], v[92:95], %[a1]\n" \
  "ds_read_b128 v[76:79], %[bp] offset:720\n" \
  "ds_read_b128 v[72:75], %[bp] offset:3280\n" \
  "v_mfma_f32_32x32x16_bf16 %[a0], v[80:83], v[88:91], %[a0]\n" \
  "ds_read2_b32 v[68:69], %[aq] offset0:144 offset1:145\n" \
  "ds_read2_b32 v[70:71], %[aq] offset0:146 offset1:147\n" \
  "v_mfma_f32_32x32x16_bf16 %[a1], v[84:87], v[88:91], %[a1]\n" \
  "ds_read2_b32 v[80:81], %[ap] offset0:152 offset1:153\n" \
  "ds_read2_b32 v[82:83], %[ap] offset0:154 offset1:155\n" \
  "s_waitcnt lgkmcnt(2)\n" \
  "v_mfma_f32_32x32x16_bf16 %[a1], v[64:67], v[76:79], %[a1]\n" \
  "ds_read_b128 v[92:95], %[bp] offset:752\n" \
  "ds_read_b128 v[88:91], %[bp] offset:3312\n" \
  "v_mfma_f32_32x32x16_bf16 %[a0], v[64:67], v[72:75], %[a0]\n" \
  "ds_read2_b32 v[84:85], %[aq] offset0:152 offset1:153\n" \
  "ds_read2_b32 v[86:87], %[aq] offset0:154 offset1:155\n" \
  "v_mfma_f32_32x32x16_bf16 %[a1], v[68:71], v[72:75], %[a1]\n" \
  "ds_read2_b32 v[64:65], %[ap] offset0:160 offset1:161\n" \
  "ds_read2_b32 v[66:67], %[ap] offset0:162 offset1:163\n" \
  "s_waitcnt lgkmcnt(2)\n" \
  "v_mfma_f32_32x32x16_bf16 %[a1], v[80:83], v[92:95], %[a1]\n" \
  "ds_read_b128 v[76:79], %[bp] offset:800\n" \
  "ds_read_b128 v[72:75], %[bp] offset:3360\n" \
  "v_mfma_f32_32x32x16_bf16 %[a0], v[80:83], v[88:91], %[a0]\n" \
  "ds_read2_b32 v[68:69], %[aq] offset0:160 offset1:161\n" \
  "ds_read2_b32 v[70:71], %[aq] offset0:162 offset1:163\n" \
  "v_mfma_f32_32x32x16_bf16 %[a1], v[84:87], v[88:91], %[a1]\n" \
  "ds_read2_b32 v[80:81], %[ap] offset0:168 offset1:169\n" \
  "ds_read2_b32 v[82:83], %[ap] offset0:170 offset1:171\n" \
  "s_waitcnt lgkmcnt(2)\n" \
  "v_mfma_f32_32x32x16_bf16 %[a1], v[64:67], v[76:79], %[a1]\n" \
  "ds_read_b128 v[92:95], %[bp] offset:832\n" \
  "ds_read_b128 v[88:91], %[bp] offset:3392\n" \
  "v_mfma_f32_32x32x16_bf16 %[a0], v[64:67], v[72:75], %[a0]\n" \
  "ds_read2_b32 v[84:85], %[aq] offset0:168 offset1:169\n" \
  "ds_read2_b32 v[86:87], %[aq] offset0:170 offset1:171\n" \
  "v_mfma_f32_32x32x16_bf16 %[a1], v[68:71], v[72:75], %[a1]\n" \
  "ds_read2_b32 v[64:65], %[ap] offset0:176 offset1:177\n" \
  "ds_read2_b32 v[66:67], %[ap] offset0:178 offset1:179\n" \
  "s_waitcnt lgkmcnt(2)\n" \
  "v_mfma_f32_32x32x16_bf16 %[a1], v[80:83], v[92:95], %[a1]\n" \
  "ds_read_b128 v[76:79], %[bp] offset:880\n" \
  "ds_read_b128 v[72:75], %[bp] offset:3440\n" \
  "v_mfma_f32_32x32x16_bf16 %[a0], v[80:83], v[88:91], %[a0]\n" \
  "ds_read2_b32 v[68:69], %[aq] offset0:176 offset1:177\n" \
  "ds_read2_b32 v[70:71], %[aq] offset0:178 offset1:179\n" \
  "v_mfma_f32_32x32x16_bf16 %[a1], v[84:87], v[88:91], %[a1]\n" \
  "ds_read2_b32 v[80:81], %[ap] offset0:184 offset1:185\n" \
  "ds_read2_b32 v[82:83], %[ap] offset0:186 offset1:187\n" \
  "s_waitcnt lgkmcnt(2)\n" \
  "v_mfma_f32_32x32x16_bf16 %[a1], v[64:67], v[76:79], %[a1]\n" \
  "ds_read_b128 v[92:95], %[bp] offset:912\n" \
  "ds_read_b128 v[88:91], %[bp] offset:3472\n" \
  "v_mfma_f32_32x32x16_bf16 %[a0], v[64:67], v[72:75], %[a0]\n" \
  "ds_read2_b32 v[84:85], %[aq] offset0:184 offset1:185\n" \
  "ds_read2_b32 v[86:87], %[aq] offset0:186 offset1:187\n" \
  "v_mfma_f32_32x32x16_bf16 %[a1], v[68:71], v[72:75], %[a1]\n" \
  "ds_read2_b32 v[64:65], %[ap] offset0:192 offset1:193\n" \
  "ds_read2_b32 v[66:67], %[ap] offset0:194 offset1:195\n" \
  "s_waitcnt lgkmcnt(2)\n" \
  "v_mfma_f32_32x32x16_bf16 %[a1], v[80:83], v[92:95], %[a1]\n" \
  "ds_read_b128 v[76:79], %[bp] offset:960\n" \
  "ds_read_b128 v[72:75], %[bp] offset:3520\n" \
  "v_mfma_f32_32x32x16_bf16 %[a0], v[80:83], v[88:91], %[a0]\n" \
  "ds_read2_b32 v[68:69], %[aq] offset0:192 offset1:193\n" \
  "ds_read2_b32 v[70:71], %[aq] offset0:194 offset1:195\n" \
  "v_mfma_f32_32x32x16_bf16 %[a1], v[84:87], v[88:91], %[a1]\n" \
  "ds_read2_b32 v[80:81], %[ap] offset0:200 offset1:201\n" \
  "ds_read2_b32 v[82:83], %[ap] offset0:202 offset1:203\n" \
  "s_waitcnt lgkmcnt(2)\n" \
  "v_mfma_f32_32x32x16_bf16 %[a1], v[64:67], v[76:79], %[a1]\n" \
  "ds_read_b128 v[92:95], %[bp] offset:992\n" \
  "ds_read_b128 v[88:91], %[bp] offset:3552\n" \
  "v_mfma_f32_32x32x16_bf16 %[a0], v[64:67], v[72:75], %[a0]\n" \
  "ds_read2_b32 v[84:85], %[aq] offset0:200 offset1:201\n" \
  "ds_read2_b32 v[86:87], %[aq] offset0:202 offset1:203\n" \
  "v_mfma_f32_32x32x16_bf16 %[a1], v[68:71], v[72:75], %[a1]\n" \
  "ds_read2_b32 v[64:65], %[ap] offset0:208 offset1:209\n" \
  "ds_read2_b32 v[66:67], %[ap] offset0:210 offset1:211\n" \
  "s_waitcnt lgkmcnt(2)\n" \
  "v_mfma_f32_32x32x16_bf16 %[a1], v[80:83], v[92:95], %[a1]\n" \
  "ds_read_b128 v[76:79], %[bp] offset:1040\n" \
  "ds_read_b128 v[72:75], %[bp] offset:3600\n" \
  "v_mfma_f32_32x32x16_bf16 %[a0], v[80:83], v[88:91], %[a0]\n" \
  "ds_read2_b32 v[68:69], %[aq] offset0:208 offset1:209\n" \
  "ds_read2_b32 v[70:71], %[aq] offset0:210 offset1:211\n" \
  "v_mfma_f32_32x32x16_bf16 %[a1], v[84:87], v[88:91], %[a1]\n" \
  "ds_read2_b32 v[80:81], %[ap] offset0:216 offset1:217\n" \
  "ds_read2_b32 v[82:83], %[ap] offset0:218 offset1:219\n" \
  "s_waitcnt lgkmcnt(2)\n" \
  "v_mfma_f32_32x32x16_bf16 %[a1], v[64:67], v[76:79], %[a1]\n" \
  "ds_read_b128 v[92:95], %[bp] offset:1072\n" \
  "ds_read_b128 v[88:91], %[bp] offset:3632\n" \
  "v_mfma_f32_32x32x16_bf16 %[a0], v[64:67], v[72:75], %[a0]\n" \
  "ds_read2_b32 v[84:85], %[aq] offset0:216 offset1:217\n" \
  "ds_read2_b32 v[86:87], %[aq] offset0:218 offset1:219\n" \
  "v_mfma_f32_32x32x16_bf16 %[a1], v[68:71], v[72:75], %[a1]\n" \
  "ds_read2_b32 v[64:65], %[ap] offset0:224 offset1:225\n" \
  "ds_read2_b32 v[66:67], %[ap] offset0:226 offset1:227\n" \
  "s_waitcnt lgkmcnt(2)\n" \
  "v_mfma_f32_32x32x16_bf16 %[a1], v[80:83], v[92:95], %[a1]\n" \
  "ds_read_b128 v[76:79], %[bp] offset:1120\n" \
  "ds_read_b128 v[72:75], %[bp] offset:3680\n" \
  "v_mfma_f32_32x32x16_bf16 %[a0], v[80:83], v[88:91], %[a0]\n" \
  "ds_read2_b32 v[68:69], %[aq] offset0:224 offset1:225\n" \
  "ds_read2_b32 v[70:71], %[aq] offset0:226 offset1:227\n" \
  "v_mfma_f32_32x32x16_bf16 %[a1], v[84:87], v[88:91], %[a1]\n" \
  "ds_read2_b32 v[80:81], %[ap] offset0:232 offset1:233\n" \
  "ds_read2_b32 v[82:83], %[ap] offset0:234 offset1:235\n" \
  "s_waitcnt lgkmcnt(2)\n" \
  "v_mfma_f32_32x32x16_bf16 %[a1], v[64:67], v[76:79], %[a1]\n" \
  "ds_read_b128 v[92:95], %[bp] offset:1152\n" \
  "ds_read_b128 v[88:91], %[bp] offset:3712\n" \
  "v_mfma_f32_32x32x16_bf16 %[a0], v[64:67], v[72:75], %[a0]\n" \
  "ds_read2_b32 v[84:85], %[aq] offset0:232 offset1:233\n" \
  "ds_read2_b32 v[86:87], %[aq] offset0:234 offset1:235\n" \
  "v_mfma_f32_32x32x16_bf16 %[a1], v[68:71], v[72:75], %[a1]\n" \
  "ds_read2_b32 v[64:65], %[ap] offset0:240 offset1:241\n" \
  "ds_read2_b32 v[66:67], %[ap] offset0:242 offset1:243\n" \
  "s_waitcnt lgkmcnt(2)\n" \
  "v_mfma_f32_32x32x16_bf16 %[a1], v[80:83], v[92:95], %[a1]\n" \
  "ds_read_b128 v[76:79], %[bp] offset:1200\n" \
  "ds_read_b128 v[72:75], %[bp] offset:3760\n" \
  "v_mfma_f32_32x32x16_bf16 %[a0], v[80:83], v[88:91], %[a0]\n" \
  "ds_read2_b32 v[68:69], %[aq] offset0:240 offset1:241\n" \
  "ds_read2_b32 v[70:71], %[aq] offset0:242 offset1:243\n" \
  "v_mfma_f32_32x32x16_bf16 %[a1], v[84:87], v[88:91], %[a1]\n" \
  "ds_read2_b32 v[80:81], %[ap] offset0:248 offset1:249\n" \
  "ds_read2_b32 v[82:83], %[ap] offset0:250 offset1:251\n" \
  "s_waitcnt lgkmcnt(2)\n" \
  "v_mfma_f32_32x32x16_bf16 %[a1], v[64:67], v[76:79], %[a1]\n" \
  "ds_read_b128 v[92:95], %[bp] offset:1232\n" \
  "ds_read_b128 v[88:91], %[bp] offset:3792\n" \
  "v_mfma_f32_32x32x16_bf16 %[a0], v[64:67], v[72:75], %[a0]\n" \
  "ds_read2_b32 v[84:85], %[aq] offset0:248 offset1:249\n" \
  "ds_read2_b32 v[86:87], %[aq] offset0:250 offset1:251\n" \
  "v_mfma_f32_32x32x16_bf16 %[a1], v[68:71], v[72:75], %[a1]\n" \
  "s_waitcnt lgkmcnt(0)\n" \
  "v_mfma_f32_32x32x16_bf16 %[a1], v[80:83], v[92:95], %[a1]\n" \
  "v_mfma_f32_32x32x16_bf16 %[a0], v[80:83], v[88:91], %[a0]\n" \
  "v_mfma_f32_32x32x16_bf16 %[a1], v[84:87], v[88:91], %[a1]\n" \
  "s_nop 15\n" \
  "s_nop 7\n"
#define CONV_LOOP_CLOBBERS "v64", "v65", "v66", "v67", "v68", "v69", "v70", "v71", "v72", "v73", "v74", "v75", "v76", "v77", "v78", "v79", "v80", "v81", "v82", "v83", "v84", "v85", "v86", "v87", "v88", "v89", "v90", "v91", "v92", "v93", "v94", "v95"
constexpr int AT_K = 0, AT_V = 65536;
constexpr int CV_H = 131072, CV_HB = 8320, CV_U = CV_H + 2 * CV_HB, CV_UB = 95 * 80  , LDS_BYTES = 163056;
constexpr int P3_WO = 49152, P3_T = 131072;
__device__ __forceinline__ constexpr int p3_o(int m) { return m < 48 ? m * 1024 : 114688 + (m - 48) * 1024; }

__device__ __forceinline__ void issue_wo(const Params& p, LAS char* lds, int wid, int lane) {
    const int ct = (blockIdx.x >> 3) & 7;
    const srd_t sw = mksrd(p.wo, D * D * 2);
    const unsigned wv = (lane >> 3) * 1024 + ((((lane & 7) >> 2) ^ ((lane >> 4) & 1)) * 64) + (lane & 3) * 16 + ct * 128;
    const unsigned l0 = (unsigned)(uintptr_t)lds + P3_WO;
#pragma unroll
    for (int i = 0; i < 8; ++i) { const int m = wid * 8 + i; dma16(sw, wv, m * 8192, l0 + m * 1024); }
}

__device__ __forceinline__ void phase2(const Params& p, LAS char* lds, unsigned long long tag) {
    char* ws = p.ws;
    const int tid = threadIdx.x, lane = tid & 63;
    const int wid = __builtin_amdgcn_readfirstlane(tid >> 6);
    const int bid = blockIdx.x, xc = bid & 7, jj = bid >> 3;
    const int i32 = lane & 31, hh = lane >> 5;
    const srd_t srd = mksrd(ws, WS_BYTES);
    STAMP(1, 0);
    if (wid >= 8) {
        const int cw = wid - 8, c2 = cw >> 1, half = cw & 1, ch = (xc * 32 + jj) * 2 + c2;
        LAS char* hb = lds + CV_H + c2 * CV_HB;
        LAS char* ub = lds + CV_U + c2 * CV_UB;
        const int t128 = half * 64 + lane;
        const u16* hrow = p.ssk + (size_t)ch * S;
        u32x4 hv[2], uv[2]; unsigned prev[2];
#pragma unroll
        for (int i = 0; i < 2; ++i) { const int t = t128 + 128 * i;
            hv[i] = *(const u32x4*)(hrow + 8 * t); prev[i] = t > 0 ? (unsigned)hrow[8 * t - 1] : 0u;
            if (!FUSED) uv[i] = ld16(srd, WS_UR + (ch * S + 8 * t) * 2); }
        const float dd = bflo((unsigned)p.ssd[ch]);
        if (FUSED) {
            if (wid == 8) { (void)poll_block<0>(ws, WS_F1B + (xc * 32 + jj) * 256, tag); (void)lds_inc(lds, CNT_U); } else lds_wait(lds, CNT_U, 1);
#pragma unroll
            for (int i = 0; i < 2; ++i) uv[i] = ld16(srd, WS_UR + (ch * S + 8 * (t128 + 128 * i)) * 2);
        }
#pragma unroll
        for (int i = 0; i < 2; ++i) { const int t = t128 + 128 * i;
            *(LAS u32x4*)(hb + (16 + 4 * t) * 4) = hv[i];
            u32x4 sv = {(hv[i][0] << 16) | prev[i], __builtin_amdgcn_alignbit(hv[i][1], hv[i][0], 16), __builtin_amdgcn_alignbit(hv[i][2], hv[i][1], 16), __builtin_amdgcn_alignbit(hv[i][3], hv[i][2], 16)};
            *(LAS u32x4*)(hb + 4160 + (16 + 4 * t) * 4) = sv;
            *(LAS u32x4*)(ub + (t >> 2) * 80 + (t & 3) * 16) = uv[i]; }
        if (t128 < 16) { *(LAS unsigned*)(hb + t128 * 4) = 0u; *(LAS unsigned*)(hb + 4160 + t128 * 4) = 0u; }
        for (int i = t128; i < 31 * 5; i += 128) *(LAS u32x4*)(ub + 64 * 80 + i * 16) = u32x4{0u, 0u, 0u, 0u};
        WAITL0(); (void)lds_inc(lds, CNT_CB + c2 * 4); lds_wait(lds, CNT_CB + c2 * 4, 2);
        STAMP(1, 1);
        const int r = i32;
        f32x16 acc0 = {}, acc1 = {};
        {
            const unsigned ap = (unsigned)(uintptr_t)hb + ((r & 1) ? 0 : 4160) + (((r + 2) >> 1) + 4 * hh) * 4 + half * 16 * 64;
            const unsigned bp = (unsigned)(uintptr_t)ub + (31 - r) * 80 + 16 * hh + half * 16 * 80;
            asm volatile(CONV_LOOP_ASM : [a0] "+v"(acc0), [a1] "+v"(acc1) : [ap] "v"(ap), [aq] "v"(ap + 2048), [bp] "v"(bp) : CONV_LOOP_CLOBBERS, "memory");
        }
        STAMP(1, 2);
        (void)lds_inc(lds, CNT_CDONE + c2 * 4);
        {
            lds_wait(lds, CNT_CDONE, 2); lds_wait(lds, CNT_CDONE + 4, 2);
            const f32x16& snd = half ? acc0 : acc1;
            LAS char* px = lds + CV_H + cw * 4096 + lane * 16;
#pragma unroll
            for (int g = 0; g < 4; ++g) *(LAS f32x4*)(px + g * 1024) = f32x4{snd[4 * g], snd[4 * g + 1], snd[4 * g + 2], snd[4 * g + 3]};
        }
        WAITL0(); (void)lds_inc(lds, CNT_CPART + c2 * 4); lds_wait(lds, CNT_CPART + c2 * 4, 2);
        {
            const f32x16& own = half ? acc1 : acc0;
            LAS const char* px = lds + CV_H + (cw ^ 1) * 4096 + lane * 16;
            const int t0 = half * 1024;
            LAS const char* ue = ub + (63 - half * 32 - r) * 80;
            LAS char* zl = lds + CV_H + (cw ^ 1) * 4096;
            f32x4 po[4];
#pragma unroll
            for (int g = 0; g < 4; ++g) po[g] = *(LAS const f32x4*)(px + g * 1024);
            WAITL0();
#pragma unroll
            for (int g = 0; g < 4; ++g) {
                const f32x4 o = po[g];
                const int rr = 8 * g + 4 * hh;
                const u32x2 uu = *(LAS const u32x2*)(ue + (28 - rr) * 2);
                const float u3 = bflo(uu[0]), u2 = bfhi(uu[0]), u1 = bflo(uu[1]), u0 = bfhi(uu[1]);
                const float z0 = rbf(own[4 * g + 0] + o[0]) + rbf(u0 * dd), z1 = rbf(own[4 * g + 1] + o[1]) + rbf(u1 * dd);
                const float z2 = rbf(own[4 * g + 2] + o[2]) + rbf(u2 * dd), z3 = rbf(own[4 * g + 3] + o[3]) + rbf(u3 * dd);
                *(LAS u32x2*)(zl + (32 * r + rr) * 2) = u32x2{cvtpk(z0, z1), cvtpk(z2, z3)};
            }
            WAITL0();
#pragma unroll
            for (int i = 0; i < 2; ++i) st16(srd, WS_ZT + (ch * S + t0) * 2 + i * 1024 + lane * 16, *(LAS const u32x4*)(zl + i * 1024 + lane * 16));
        }
        STAMP(1, 4);
        if (FUSED) { WAITV(0); if (lds_inc(lds, CNT_Z) == 3 && lane < 32) put_flag(ws, WS_F2B + ((xc * 32 + lane) * 32 + jj) * 8, tag); }
        return;
    }
    const int head = xc * 4 + (jj >> 3), qb = jj & 7;
    const int qp = wid & 3, kh = wid >> 2;
    const int qt = qb * 8 + 2 * qp;
    if (wid == 0) {
        const unsigned fo = WS_F1A + (head * 8 + qb) * 256;
        const unsigned long long v = FUSED ? (poll_block<24>(ws, fo, tag << 24 >> 24) ) : __hip_atomic_load((gu64*)(ws + fo) + (lane & 31), __ATOMIC_RELAXED, __HIP_MEMORY_SCOPE_AGENT);
        float kn = decode12((unsigned)(v >> 12) & 0xfffu), qn = (((lane & 31) >> 2) == qb) ? decode12((unsigned)v & 0xfffu) : 0.f;
        kn = dppmax<0x143, 0xc>(dppmax<0x142, 0xa>(rowmax16(kn))); qn = dppmax<0x143, 0xc>(dppmax<0x142, 0xa>(rowmax16(qn)));
        if (lane == 63) *(LAS int*)(lds + FAST_LDS) = (kn * qn * 1.02f <= FAST_BOUND2) ? 1 : 0;
        WAITL0();
        if (FUSED) lds_wait(lds, CNT_QKV, 4);
        (void)lds_inc(lds, CNT_GO1);
    } else lds_wait(lds, CNT_GO1, 1);
    STAMP(1, 5);
    const bool fast = __builtin_amdgcn_readfirstlane(*(LAS const int*)(lds + FAST_LDS)) != 0;
    const unsigned fill = (i32 == 16 || i32 == 20) ? 0x3F803F80u : 0u;
    const unsigned oq = WS_QF + ((head * 64 + qt) * 64 + lane) * 16;
    const unsigned voff = wid * 1024 + lane * 16, l0 = (unsigned)(uintptr_t)lds + wid * 1024, kb = WS_KF + head * 65536;
    f32x16 o1 = {}, o2 = {};
    STAMP(1, 1); STAMPC(1, 1);
    if (fast) {
        unsigned ka = (unsigned)(uintptr_t)lds + AT_K + kh * 32768 + lane * 16;
        unsigned va = (unsigned)(uintptr_t)lds + AT_V + kh * 32768 + hh * 256 + (i32 & 15) * 16;
        unsigned g, t;
        const unsigned long long vm = 0x0000FFFF0000FFFFull;
        const unsigned cnt = (unsigned)(uintptr_t)lds + CNT_KV;
        asm volatile(ATTN_LOOP_ASM : [o1] "+v"(o1), [o2] "+v"(o2), [ka] "+v"(ka), [va] "+v"(va), [g] "=&s"(g), [t] "=&s"(t)
                     : [cnt] "v"(cnt), [qo] "v"(oq), [vo] "v"(voff), [l0] "s"(l0), [kb] "s"(kb), [srd] "s"(srd), [vm] "s"(vm), [fill] "v"(fill), [w0] "n"(12), [w1] "n"(8)
                     : ATTN_LOOP_CLOBBERS, "scc", "memory");
    } else {
        const bf16x8 q1 = __builtin_bit_cast(bf16x8, ld16(srd, oq)), q2 = __builtin_bit_cast(bf16x8, ld16(srd, oq + 1024));
#pragma unroll
        for (int c = 0; c < 8; ++c) { dma16c(srd, voff, kb + c * 8192, l0 + AT_K + c * 8192); dma16c(srd, voff, kb + (WS_VF - WS_KF) + c * 8192, l0 + AT_V + c * 8192); }
        WAITV(0); (void)lds_inc(lds, CNT_KV); lds_wait(lds, CNT_KV, 8);
        const bool real = i32 < 16;
        const bf16x8 fl = __builtin_bit_cast(bf16x8, u32x4{fill, fill, fill, fill});
        float m1 = -INFINITY, m2 = -INFINITY;
        for (int kt = 0; kt < 64; ++kt) {
            const bf16x8 kf = *(LAS const bf16x8*)(lds + AT_K + kt * 1024 + lane * 16);
            const f32x16 s1 = MFMA32(kf, q1, (f32x16){}), s2 = MFMA32(kf, q2, (f32x16){});
#pragma unroll
            for (int r = 0; r < 16; ++r) { m1 = __builtin_fmaxf(m1, s1[r]); m2 = __builtin_fmaxf(m2, s2[r]); }
        }
        m1 = __builtin_fmaxf(m1, swap32(m1)); m2 = __builtin_fmaxf(m2, swap32(m2));
        if (m1 == -INFINITY) m1 = 0.f;
        if (m2 == -INFINITY) m2 = 0.f;
        for (int kt = kh * 32; kt < kh * 32 + 32; ++kt) {
            const bf16x8 kf = *(LAS const bf16x8*)(lds + AT_K + kt * 1024 + lane * 16);
            bf16x8 v0 = *(LAS const bf16x8*)(lds + AT_V + kt * 1024 + hh * 256 + (i32 & 15) * 16);
            bf16x8 v1 = *(LAS const bf16x8*)(lds + AT_V + kt * 1024 + 512 + hh * 256 + (i32 & 15) * 16);
            v0 = real ? v0 : fl; v1 = real ? v1 : fl;
            f32x16 s1 = MFMA32(kf, q1, (f32x16){}), s2 = MFMA32(kf, q2, (f32x16){});
#pragma unroll
            for (int r = 0; r < 16; ++r) { s1[r] = __builtin_amdgcn_exp2f(s1[r] - m1); s2[r] = __builtin_amdgcn_exp2f(s2[r] - m2); }
            o1 = smf(v0, lo8(s1), o1); o1 = smf(v1, hi8(s1), o1);
            o2 = smf(v0, lo8(s2), o2); o2 = smf(v1, hi8(s2), o2);
        }
    }
    STAMP(1, 3); STAMPC(1, 3);
    (void)lds_inc(lds, CNT_ADONE); lds_wait(lds, CNT_ADONE, 8);
    {
        const f32x16& snd = kh ? o1 : o2;
        LAS char* px = lds + wid * 4096 + lane * 16;
        *(LAS f32x4*)(px) = f32x4{snd[0], snd[1], snd[2], snd[3]};
        *(LAS f32x4*)(px + 1024) = f32x4{snd[4], snd[5], snd[6], snd[7]};
        *(LAS float*)(lds + wid * 4096 + 2048 + lane * 4) = snd[8];
    }
    WAITL0(); (void)lds_inc(lds, CNT_APART); lds_wait(lds, CNT_APART, 8);
    {
        const f32x16& own = kh ? o2 : o1;
        LAS const char* px = lds + (wid ^ 4) * 4096 + lane * 16;
        const f32x4 a = *(LAS const f32x4*)(px), b = *(LAS const f32x4*)(px + 1024);
        const float l = own[8] + *(LAS const float*)(lds + (wid ^ 4) * 4096 + 2048 + lane * 4);
        const float inv = 1.0f / l;
        st16(srd, WS_OF + (((qt + kh) * 32 + head) * 64 + lane) * 16,
             u32x4{cvtpk((own[0] + a[0]) * inv, (own[1] + a[1]) * inv), cvtpk((own[2] + a[2]) * inv, (own[3] + a[3]) * inv),
                   cvtpk((own[4] + b[0]) * inv, (own[5] + b[1]) * inv), cvtpk((own[6] + b[2]) * inv, (own[7] + b[3]) * inv)});
    }
    STAMP(1, 4);
    if (FUSED) {
        issue_wo(p, lds, wid, lane);
        WAITV(8);
        if (lds_inc(lds, CNT_O) == 7 && lane < 32) { put_flag(ws, WS_F2A + ((qb * 32 + lane) * 32 + head) * 8, tag); STAMP(1, 6); }
    }
}

__device__ __forceinline__ void phase3(const Params& p, LAS char* lds, unsigned long long tag) {
    char* ws = p.ws;
    const int tid = threadIdx.x, lane = tid & 63;
    const int wid = __builtin_amdgcn_readfirstlane(tid >> 6);
    const int bid = blockIdx.x, xc = bid & 7, jj = bid >> 3;
    const srd_t srd = mksrd(ws, WS_BYTES);
    const int rt = xc * 4 + (jj >> 3), ct = jj & 7;
    STAMP(2, 0);
    if (wid >= 8) {
        const int cw = wid - 8, st = rt, d0 = ct * 64 + cw * 16;
        LAS char* tl = lds + CV_H + (cw ^ 1) * 4096;
        u32x4 z[2], qv[2];
#pragma unroll
        for (int i = 0; i < 2; ++i) { const int e = lane + 64 * i; qv[i] = *(const u32x4*)(p.q + (size_t)(st * 64 + (e >> 1)) * D + d0 + (e & 1) * 8); }
        if (FUSED) { if (wid == 8) { (void)poll_block<0>(ws, WS_F2B + (ct * 32 + rt) * 256, tag); (void)lds_inc(lds, CNT_ZGO); } else lds_wait(lds, CNT_ZGO, 1); }
#pragma unroll
        for (int i = 0; i < 2; ++i) { const int e = lane + 64 * i; z[i] = ld16(srd, WS_ZT + ((d0 + (e >> 3)) * S + st * 64 + (e & 7) * 8) * 2); }
#pragma unroll
        for (int i = 0; i < 2; ++i) { const int e = lane + 64 * i; *(LAS u32x4*)(tl + (e >> 3) * 144 + (e & 7) * 16) = z[i]; }
        WAITL0();
#pragma unroll
        for (int i = 0; i < 2; ++i) { const int e = lane + 64 * i, sl = e >> 1, c = (e & 1) * 8;
            u32x4 ov;
#pragma unroll
            for (int j = 0; j < 4; ++j) { const float z0 = bflo((unsigned)*(LAS const u16*)(tl + (c + 2 * j) * 144 + sl * 2)), z1 = bflo((unsigned)*(LAS const u16*)(tl + (c + 2 * j + 1) * 144 + sl * 2));
                ov[j] = cvtpk(bflo(qv[i][j]) * z0, bfhi(qv[i][j]) * z1); }
            *(u32x4*)(p.out + (size_t)(st * 64 + sl) * (2 * D) + d0 + c) = ov; }
        STAMP(2, 3);
        return;
    }
    if (FUSED) {
        if (wid == 0) {
            lds_wait(lds, CNT_O, 8);
            (void)poll_block<0>(ws, WS_F2A + ((rt >> 2) * 32 + (rt & 3) * 8 + ct) * 256, tag);
            if (lane == 0) { __hip_atomic_store((LAS unsigned*)(lds + CNT_GO), 1u, __ATOMIC_RELAXED, __HIP_MEMORY_SCOPE_WORKGROUP);
                             if (bid == 0) put_flag(ws, WS_EPOCH, tag); }
        } else lds_wait(lds, CNT_GO, 1);
        STAMP(2, 4);
    } else issue_wo(p, lds, wid, lane);
    {
        const unsigned l0 = (unsigned)(uintptr_t)lds;
#pragma unroll
        for (int i = 0; i < 8; ++i) { const int m = wid * 8 + i;
            dma16c(srd, lane * 16, WS_OF + (rt * 2 * 32 + m) * 1024, l0 + (m < 48 ? m * 1024 : 114688 + (m - 48) * 1024)); }
    }
    WAITV(0);
    (void)lds_inc(lds, CNT_LAND);
    if (wid >= 4) return;
    lds_wait(lds, CNT_LAND, 8);
    STAMP(2, 1);
    const int wr = wid >> 1, wc = wid & 1, hh = lane >> 5, qrow = (lane & 15) >> 2;
    LAS const char* wp = lds + P3_WO + (4 * hh + qrow) * 128 + ((wc ^ (qrow >> 1)) * 64) + ((lane >> 4) & 1) * 32 + (lane & 3) * 8;
    LAS const char* opa = lds + wr * 32768 + lane * 16;
    LAS const char* opb = lds + (wr ? 114688 : 16384) + lane * 16;
    f32x16 acc = {}, acc2 = {};
#pragma unroll
    for (int h = 0; h < 16; ++h) {
        const bf16x8 wf = cat8(trread(wp + h * 2048), trread(wp + h * 2048 + 1024)), of = *(LAS const bf16x8*)(opa + h * 1024);
        const bf16x8 wf2 = cat8(trread(wp + 32768 + h * 2048), trread(wp + 32768 + h * 2048 + 1024)), of2 = *(LAS const bf16x8*)(opb + h * 1024);
        acc = MFMA32(wf, of, acc);
        acc2 = MFMA32(wf2, of2, acc2);
    }
    __builtin_amdgcn_sched_group_barrier(0x100, 12, 0);
#pragma unroll
    for (int i = 0; i < 28; ++i) { __builtin_amdgcn_sched_group_barrier(0x008, 1, 0); __builtin_amdgcn_sched_group_barrier(0x100, 3, 0); }
    __builtin_amdgcn_sched_group_barrier(0x008, 4, 0);
    STAMP(2, 2);
    unsigned pk[8];
#pragma unroll
    for (int j = 0; j < 8; ++j) pk[j] = cvtpk(acc[2 * j] + acc2[2 * j], acc[2 * j + 1] + acc2[2 * j + 1]);
    u16* orow = p.out + (size_t)((rt * 2 + wr) * 32 + (lane & 31)) * (2 * D) + D + ct * 64 + wc * 32 + hh * 8;
#pragma unroll
    for (int g = 0; g < 4; g += 2) {
        auto s0 = __builtin_amdgcn_permlane32_swap(pk[2 * g], pk[2 * g + 2], false, false);
        auto s1 = __builtin_amdgcn_permlane32_swap(pk[2 * g + 1], pk[2 * g + 3], false, false);
        *(u32x4*)(orow + g * 8) = u32x4{s0[0], s1[0], s0[1], s1[1]};
    }
    STAMP(2, 3);
}

__global__ __launch_bounds__(768, 3) void k_all(Params p) {
    extern __shared__ __attribute__((aligned(16))) char smem[];
    asm volatile("" :: "s"(p.q), "s"(p.k), "s"(p.v), "s"(p.shk), "s"(p.ssk), "s"(p.ssd), "s"(p.x), "s"(p.wqkv), "s"(p.wo), "s"(p.out), "s"(p.ws));
    unsigned long long tag = 0;
    if (threadIdx.x < N_CNT) *(LAS unsigned*)((LAS char*)smem + FAST_LDS + threadIdx.x * 4) = 0u;
    phase1(p, (LAS char*)smem, tag);
    phase2(p, (LAS char*)smem, tag);
    phase3(p, (LAS char*)smem, tag);
}

extern "C" void kernel_launch(void* const* d_in, const int* in_sizes, int n_in, void* d_out, int out_size, void* d_ws, size_t ws_size, hipStream_t stream) {
    Params p;
    p.q = (const u16*)d_in[0]; p.k = (const u16*)d_in[1]; p.v = (const u16*)d_in[2];
    p.shk = (const u16*)d_in[3]; p.ssk = (const u16*)d_in[4]; p.ssd = (const u16*)d_in[5];
    p.x = (const u16*)d_in[6]; p.wqkv = (const u16*)d_in[7]; p.wo = (const u16*)d_in[8];
    p.out = (u16*)d_out; p.ws = (char*)d_ws;
    (void)hipFuncSetAttribute((const void*)k_all, hipFuncAttributeMaxDynamicSharedMemorySize, LDS_BYTES);
    k_all<<<256, 768, LDS_BYTES, stream>>>(p);
}
```

```cpp
#include <hip/hip_runtime.h>
#include <hip/hip_bf16.h>
#include <stdint.h>

typedef __bf16 bf16x8 __attribute__((ext_vector_type(8)));
typedef __bf16 bf16x16 __attribute__((ext_vector_type(16)));
typedef short s16x4 __attribute__((ext_vector_type(4)));
typedef float f32x16 __attribute__((ext_vector_type(16)));
typedef float f32x8 __attribute__((ext_vector_type(8)));
typedef float f32x4 __attribute__((ext_vector_type(4)));
typedef unsigned u32x4 __attribute__((ext_vector_type(4)));
typedef unsigned u32x2 __attribute__((ext_vector_type(2)));
typedef unsigned short u16;
#define LAS __attribute__((address_space(3)))
#define GAS __attribute__((address_space(1)))
typedef __amdgpu_buffer_rsrc_t srd_t;

constexpr int S = 2048, D = 512;
constexpr float QSCALE = 0.25f * 1.4426950408889634f;
constexpr float FAST_BOUND2 = 60.f * 60.f;

constexpr unsigned WS_QF = 0, WS_KF = 2u << 20, WS_VF = 4u << 20, WS_OF = 6u << 20, WS_UR = 8u << 20, WS_ZT = 10u << 20,
                   WS_F1A = 12u << 20,
                   WS_F1B = WS_F1A + 65536,
                   WS_F2A = WS_F1B + 65536,
                   WS_F2B = WS_F2A + 65536,
                   WS_EPOCH = WS_F2B + 65536,
                   WS_BYTES = 13u << 20;
constexpr int AUX = 16;
#define SC1 " sc1"
constexpr int POLL_SLEEP = 4, PRIO_GROUP = 1, CONV_PRIO = 3;

#define MFMA32(a, b, c) __builtin_amdgcn_mfma_f32_32x32x16_bf16(a, b, c, 0, 0, 0)
#define BAR() asm volatile("s_barrier" ::: "memory")
#define WAITV(n) asm volatile("s_waitcnt vmcnt(" #n ")" ::: "memory")
#define WAITL0() asm volatile("s_waitcnt lgkmcnt(0)" ::: "memory")
#define SB() __builtin_amdgcn_sched_barrier(0)

__device__ __forceinline__ unsigned cvtpk(float lo, float hi) {
    typedef __bf16 bf2 __attribute__((ext_vector_type(2)));
    bf2 r; r[0] = (__bf16)lo; r[1] = (__bf16)hi; return __builtin_bit_cast(unsigned, r);
}
__device__ __forceinline__ float bflo(unsigned u) { return __uint_as_float(u << 16); }
__device__ __forceinline__ float bfhi(unsigned u) { return __uint_as_float(u & 0xffff0000u); }
__device__ __forceinline__ float rbf(float f) { return __uint_as_float(cvtpk(f, 0.f) << 16); }
__device__ __forceinline__ s16x4 trread(LAS const char* p) {
    typedef short v4i16_t __attribute__((ext_vector_type(4)));
    return __builtin_amdgcn_ds_read_tr16_b64_v4i16((LAS v4i16_t*)p);
}
__device__ __forceinline__ bf16x8 cat8(s16x4 a, s16x4 b) {
    typedef short s16x8 __attribute__((ext_vector_type(8)));
    s16x8 r = __builtin_shufflevector(a, b, 0, 1, 2, 3, 4, 5, 6, 7);
    return __builtin_bit_cast(bf16x8, r);
}
__device__ __forceinline__ f32x16 smf(bf16x8 a, f32x8 b, f32x16 c) {
    return __builtin_amdgcn_smfmac_f32_32x32x32_bf16(a, __builtin_bit_cast(bf16x16, b), c, 0xDDDD, 0, 0);
}
__device__ __forceinline__ f32x8 lo8(f32x16 x) { return __builtin_shufflevector(x, x, 0, 1, 2, 3, 4, 5, 6, 7); }
__device__ __forceinline__ f32x8 hi8(f32x16 x) { return __builtin_shufflevector(x, x, 8, 9, 10, 11, 12, 13, 14, 15); }
__device__ __forceinline__ float swap32(float x) {
    auto r = __builtin_amdgcn_permlane32_swap(__float_as_uint(x), __float_as_uint(x), false, false);
    return __uint_as_float((threadIdx.x & 32) ? r[0] : r[1]);
}
template <int CTRL, int RM> __device__ __forceinline__ float dppmax(float v) {
    return __builtin_fmaxf(v, __builtin_bit_cast(float, __builtin_amdgcn_update_dpp(0, __builtin_bit_cast(int, v), CTRL, RM, 0xf, true)));
}
__device__ __forceinline__ float rowmax16(float v) { v = dppmax<0x111, 0xf>(v); v = dppmax<0x112, 0xf>(v); v = dppmax<0x114, 0xf>(v); return dppmax<0x118, 0xf>(v); }
__device__ __forceinline__ void dma16(srd_t srd, unsigned voff, unsigned soff, unsigned ldsaddr) {
    asm volatile("s_mov_b32 m0, %0\n\ts_nop 0\n\tbuffer_load_dwordx4 %1, %2, %3 offen lds" :: "s"(ldsaddr), "v"(voff), "s"(srd), "s"(soff) : "memory");
}
__device__ __forceinline__ void dma16c(srd_t srd, unsigned voff, unsigned soff, unsigned ldsaddr) {
    asm volatile("s_mov_b32 m0, %0\n\ts_nop 0\n\tbuffer_load_dwordx4 %1, %2, %3 offen" SC1 " lds" :: "s"(ldsaddr), "v"(voff), "s"(srd), "s"(soff) : "memory");
}
__device__ __forceinline__ void st16(srd_t srd, unsigned off, u32x4 v) { __builtin_amdgcn_raw_buffer_store_b128(v, srd, off, 0, AUX); }
__device__ __forceinline__ u32x4 ld16(srd_t srd, unsigned off) { return __builtin_amdgcn_raw_buffer_load_b128(srd, off, 0, AUX); }
typedef GAS unsigned long long gu64;
template <int SH> __device__ __forceinline__ unsigned long long poll_block(char* ws, unsigned off, unsigned long long tag) {
    gu64* w = (gu64*)(ws + off) + (threadIdx.x & 31);
    const unsigned long long want = (tag << SH) >> SH;
    unsigned long long v = 0;
#pragma unroll 1
    for (unsigned spins = 0; spins < (1u << 16); ++spins) {
        v = __hip_atomic_load(w, __ATOMIC_RELAXED, __HIP_MEMORY_SCOPE_AGENT);
        if (__all((v >> SH) == want)) break;
        __builtin_amdgcn_s_sleep(POLL_SLEEP);
    }
    asm volatile("" ::: "memory");
    return v;
}
__device__ __forceinline__ unsigned code12(float v) { const unsigned u = (__float_as_uint(v) + 0x7ffffu) >> 19; return u > 0xff0u ? 0xff0u : u; }
__device__ __forceinline__ float decode12(unsigned c) { return __uint_as_float(c << 19); }
__device__ __forceinline__ unsigned long long tag_from_lds(LAS char* lds, int off) {
    const u32x2 e = *(LAS const u32x2*)(lds + off);
    return (((unsigned long long)__builtin_amdgcn_readfirstlane(e[1]) << 32) | (unsigned)__builtin_amdgcn_readfirstlane(e[0])) + 1ull;
}
__device__ __forceinline__ void lds_add(LAS char* lds, int off) {
    if ((threadIdx.x & 63) == 0) (void)__hip_atomic_fetch_add((LAS unsigned*)(lds + off), 1u, __ATOMIC_RELAXED, __HIP_MEMORY_SCOPE_WORKGROUP);
}
__device__ __forceinline__ unsigned lds_inc(LAS char* lds, int off) {
    unsigned old = 0;
    if ((threadIdx.x & 63) == 0) old = __hip_atomic_fetch_add((LAS unsigned*)(lds + off), 1u, __ATOMIC_RELAXED, __HIP_MEMORY_SCOPE_WORKGROUP);
    return __builtin_amdgcn_readfirstlane(old);
}
template <int SL = 1> __device__ __forceinline__ void lds_wait(LAS char* lds, int off, unsigned want) {
#pragma unroll 1
    for (unsigned spins = 0; spins < (1u << 22); ++spins) {
        if (__builtin_amdgcn_readfirstlane(__hip_atomic_load((LAS unsigned*)(lds + off), __ATOMIC_RELAXED, __HIP_MEMORY_SCOPE_WORKGROUP)) >= want) break;
        __builtin_amdgcn_s_sleep(SL);
    }
    asm volatile("" ::: "memory");
}
__device__ __forceinline__ void put_flag(char* ws, unsigned off, unsigned long long tag) { __hip_atomic_store((gu64*)(ws + off), tag, __ATOMIC_RELAXED, __HIP_MEMORY_SCOPE_AGENT); }
__device__ __forceinline__ srd_t mksrd(const void* p, unsigned bytes) { return __builtin_amdgcn_make_buffer_rsrc((void*)p, (short)0, (int)bytes, 0x00020000); }

struct Params {
    const u16 *q, *k, *v, *shk, *ssk, *ssd, *x, *wqkv, *wo;
    u16* out; char* ws;
};

constexpr int P1_STAGE = 32768, P1_NST = 4, T1_LDS = 131072, NRM_LDS = 162912, FAST_LDS = NRM_LDS + 64;
constexpr int CNT_QKV = FAST_LDS + 4, CNT_O = FAST_LDS + 8, CNT_Z = FAST_LDS + 12, CNT_GO = FAST_LDS + 16, CNT_LAND = FAST_LDS + 20, CNT_U = FAST_LDS + 24, CNT_ZGO = FAST_LDS + 28,
              CNT_GO1 = FAST_LDS + 32, CNT_ADONE = FAST_LDS + 36, CNT_APART = FAST_LDS + 40, CNT_T1 = FAST_LDS + 44,
              CNT_CB = FAST_LDS + 48, CNT_CDONE = FAST_LDS + 56, CNT_CPART = FAST_LDS + 64,
              CNT_KV = FAST_LDS + 72  , CNT_PUB = CNT_KV + 20, CNT_TX = CNT_KV + 28, N_CNT = 28, TAG_LDS = FAST_LDS + 112;

#define P1_DMA_SETUP(PW) const int pw = (PW); \
        const srd_t sx = mksrd(p.x, S * D * 2), sw = mksrd(p.wqkv, D * 3 * D * 2); \
        unsigned xo[2], wv[2]; \
        _Pragma("unroll") for (int i = 0; i < 2; ++i) { const int row = 8 * (2 * pw + i) + (lane >> 3), c = (lane & 7) ^ ((row >> 1) & 7); xo[i] = ((rb * 64 + row) * D + c * 8) * 2; \
            wv[i] = row * 3072 + ((((lane & 7) >> 2) ^ ((lane >> 4) & 1)) * 64) + (lane & 3) * 16 + g4 * 128; } \
        const unsigned l0 = (unsigned)(uintptr_t)lds + pw * 2048; \
        auto issue = [&](int c) { \
            const unsigned st = l0 + (c % P1_NST) * P1_STAGE; const int cc = (c + rot) & 7; \
            const unsigned sox = cc * 128, sow = cc * 196608; \
            asm volatile("s_mov_b32 m0, %[st]\n\ts_nop 0\n\tbuffer_load_dwordx4 %[x0], %[sx], %[sox] offen lds\n\t" \
                         "s_add_u32 m0, %[st], 1024\n\ts_nop 0\n\tbuffer_load_dwordx4 %[x1], %[sx], %[sox] offen lds\n\t" \
                         "s_add_u32 m0, %[st], 8192\n\ts_nop 0\n\tbuffer_load_dwordx4 %[w0], %[sw], %[sow] offen lds\n\t" \
                         "s_add_u32 m0, %[st], 9216\n\ts_nop 0\n\tbuffer_load_dwordx4 %[w1], %[sw], %[sow] offen lds\n\t" \
                         "s_add_u32 m0, %[st], 15360\n\ts_nop 0\n\tbuffer_load_dwordx4 %[w0], %[sw], %[sow] offen offset:1024 lds\n\t" \
                         "s_add_u32 m0, %[st], 16384\n\ts_nop 0\n\tbuffer_load_dwordx4 %[w1], %[sw], %[sow] offen offset:1024 lds\n\t" \
                         "s_add_u32 m0, %[st], 22528\n\ts_nop 0\n\tbuffer_load_dwordx4 %[w0], %[sw], %[sow] offen offset:2048 lds\n\t" \
                         "s_add_u32 m0, %[st], 23552\n\ts_nop 0\n\tbuffer_load_dwordx4 %[w1], %[sw], %[sow] offen offset:2048 lds" \
                         :: [st] "s"(st), [x0] "v"(xo[0]), [x1] "v"(xo[1]), [w0] "v"(wv[0]), [w1] "v"(wv[1]), [sx] "s"(sx), [sw] "s"(sw), [sox] "s"(sox), [sow] "s"(sow) : "scc", "memory"); \
        };
__device__ __forceinline__ void phase1(const Params& p, LAS char* lds, unsigned long long& tag) {
    char* ws = p.ws;
    const int tid = threadIdx.x, lane = tid & 63;
    const int wid = __builtin_amdgcn_readfirstlane(tid >> 6);
    const int bid = blockIdx.x, xc = bid & 7, jj = bid >> 3;
    const int rb = (xc & 3) * 8 + (jj & 7), g4 = (xc >> 2) * 4 + (jj >> 3);
    const int rot = ((jj & 7) + 2 * (jj >> 3) + xc) & 7;
    const srd_t sws = mksrd(ws, WS_BYTES);

    if (wid >= 8) {
        const int slab = bid >> 3, cg = bid & 7;
        const int sl = (wid - 8) * 16 + (lane >> 5) * 8, s0 = slab * 64 + sl, cl = (lane & 31) * 2, ch = cg * 64 + cl;
        const u32x2 ev = __builtin_amdgcn_raw_buffer_load_b64(sws, WS_EPOCH, 0, AUX);
        BAR();
        SB();
        unsigned kk[11], vv[8];
        const srd_t sk = mksrd(p.k, S * D * 2), sv = mksrd(p.v, S * D * 2);
        const int ko = ((s0 - 3) * D + ch) * 2;
#pragma unroll
        for (int i = 0; i < 11; ++i) kk[i] = __builtin_amdgcn_raw_buffer_load_b32(sk, (s0 - 3 + i >= 0) ? ko + i * D * 2 : 0x7ffffff0, 0, 0);
#pragma unroll
        for (int i = 0; i < 8; ++i) vv[i] = __builtin_amdgcn_raw_buffer_load_b32(sv, ((s0 + i) * D + ch) * 2, 0, 0);
        const u32x4 sw = *(const u32x4*)(p.shk + ch * 4);
        SB();
        BAR(); BAR();
        if (wid == 8 && lane == 0) *(LAS u32x2*)(lds + TAG_LDS) = ev;
        WAITL0(); BAR();
        tag = tag_from_lds(lds, TAG_LDS);
        BAR(); BAR(); BAR(); BAR();
        SB();

        float w0[4], w1[4];
        w0[0] = bflo(sw[0]); w0[1] = bfhi(sw[0]); w0[2] = bflo(sw[1]); w0[3] = bfhi(sw[1]);
        w1[0] = bflo(sw[2]); w1[1] = bfhi(sw[2]); w1[2] = bflo(sw[3]); w1[3] = bfhi(sw[3]);
        float r0[8], r1[8];
#pragma unroll
        for (int i = 0; i < 8; ++i) {
            float a0 = bflo(kk[i]) * w0[3], a1 = bfhi(kk[i]) * w1[3];
            a0 += bflo(kk[i + 1]) * w0[2]; a1 += bfhi(kk[i + 1]) * w1[2];
            a0 += bflo(kk[i + 2]) * w0[1]; a1 += bfhi(kk[i + 2]) * w1[1];
            a0 += bflo(kk[i + 3]) * w0[0]; a1 += bfhi(kk[i + 3]) * w1[0];
            r0[i] = rbf(a0) * bflo(vv[i]); r1[i] = rbf(a1) * bfhi(vv[i]);
        }
        LAS char* tb = lds + T1_LDS;
        *(LAS u32x4*)(tb + cl * 144 + (7 - (sl >> 3)) * 16) = u32x4{cvtpk(r0[7], r0[6]), cvtpk(r0[5], r0[4]), cvtpk(r0[3], r0[2]), cvtpk(r0[1], r0[0])};
        *(LAS u32x4*)(tb + (cl + 1) * 144 + (7 - (sl >> 3)) * 16) = u32x4{cvtpk(r1[7], r1[6]), cvtpk(r1[5], r1[4]), cvtpk(r1[3], r1[2]), cvtpk(r1[1], r1[0])};
        WAITL0(); lds_add(lds, CNT_TX); lds_wait(lds, CNT_TX, 4);
#pragma unroll
        for (int i = 0; i < 2; ++i) { const int row = (wid - 8) * 16 + i * 8 + (lane >> 3);
            st16(sws, WS_UR + ((cg * 64 + row) * S + (S - 64 - slab * 64)) * 2 + (lane & 7) * 16, *(LAS const u32x4*)(tb + row * 144 + (lane & 7) * 16)); }

        WAITV(0);
        if (lds_inc(lds, CNT_T1) == 3 && lane < 32) put_flag(ws, WS_F1B + ((cg * 32 + lane) * 32 + slab) * 8, tag);
        return;
    }
    if (wid >= 4) {
        P1_DMA_SETUP(wid - 4);

        issue(0); issue(1); issue(2); issue(3);
        WAITV(24); BAR();
        WAITV(16); BAR(); issue(4);
        WAITV(16); BAR(); issue(5);
        WAITV(16); BAR(); issue(6);
        WAITV(16); BAR(); issue(7);
        tag = tag_from_lds(lds, TAG_LDS);
        WAITV(16); BAR();
        WAITV(8); BAR();
        WAITV(0);
        BAR();

        return;
    }
    const int i32 = lane & 31, hh = lane >> 5;
    const int tl = wid & 1, cf = wid >> 1;
    const int row = tl * 32 + i32, swz = (row >> 1) & 7;
    int xoff[4];
#pragma unroll
    for (int s = 0; s < 4; ++s) xoff[s] = row * 128 + (((2 * s + hh) ^ swz) * 16);
    const int qrow = (lane & 15) >> 2;
    const int woff = 8192 + (8 * hh + qrow) * 128 + ((cf ^ (qrow >> 1)) * 64) + ((lane >> 4) & 1) * 32 + (lane & 3) * 8;
    f32x16 aq = {}, ak = {}, av = {};
    bf16x8 xa[2], qa[2], ka[2], va[2], xb[2], qb[2], kb[2], vb[2];
#define P1_RD(X, Q, K, V, st, s0) do { _Pragma("unroll") for (int s_ = 0; s_ < 2; ++s_) { \
        LAS const char* wp_ = (st) + woff + ((s0) + s_) * 2048; \
        X[s_] = *(LAS const bf16x8*)((st) + xoff[(s0) + s_]); \
        Q[s_] = cat8(trread(wp_), trread(wp_ + 512)); K[s_] = cat8(trread(wp_ + 8192), trread(wp_ + 8192 + 512)); V[s_] = cat8(trread(wp_ + 16384), trread(wp_ + 16384 + 512)); } } while (0)
#define P1_MM(X, Q, K, V) do { _Pragma("unroll") for (int s_ = 0; s_ < 2; ++s_) { aq = MFMA32(Q[s_], X[s_], aq); ak = MFMA32(K[s_], X[s_], ak); av = MFMA32(X[s_], V[s_], av); } } while (0)
#define P1_MIX() do { _Pragma("unroll") for (int i_ = 0; i_ < 4; ++i_) { __builtin_amdgcn_sched_group_barrier(0x008, 1, 0); __builtin_amdgcn_sched_group_barrier(0x100, 3, 0); } \
        __builtin_amdgcn_sched_group_barrier(0x008, 1, 0); __builtin_amdgcn_sched_group_barrier(0x100, 2, 0); __builtin_amdgcn_sched_group_barrier(0x008, 1, 0); } while (0)

#pragma unroll
    for (int c = 0; c < 8; ++c) {
        BAR();

        LAS const char* st = lds + (c % P1_NST) * P1_STAGE;
        P1_RD(xa, qa, ka, va, st, 0);
        if (c > 0) { P1_MM(xb, qb, kb, vb); P1_MIX(); }
        SB();
        P1_RD(xb, qb, kb, vb, st, 2);
        P1_MM(xa, qa, ka, va); P1_MIX();
        SB();
        WAITL0();
    }

    P1_MM(xb, qb, kb, vb);

    tag = tag_from_lds(lds, TAG_LDS);
    const int tt = rb * 2 + tl;
    const int h0 = g4 * 4 + cf * 2;
    {
        const int e = i32 >> 4, d = i32 & 15;
#pragma unroll
        for (int sub = 0; sub < 2; ++sub)
            st16(sws, WS_VF + (((h0 + e) * 64 + tt) * 2 + sub) * 512 + hh * 256 + d * 16,
                 u32x4{cvtpk(av[8 * sub + 0], av[8 * sub + 1]), cvtpk(av[8 * sub + 2], av[8 * sub + 3]), cvtpk(av[8 * sub + 4], av[8 * sub + 5]), cvtpk(av[8 * sub + 6], av[8 * sub + 7])});
    }
    {
        const int pi = (i32 & 19) | ((i32 & 4) << 1) | ((i32 & 8) >> 1);
#pragma unroll
        for (int e = 0; e < 2; ++e) {
            float q8[8], k8[8]; float sq = 0.f, sk = 0.f;
#pragma unroll
            for (int j = 0; j < 8; ++j) { q8[j] = aq[8 * e + j] * QSCALE; k8[j] = ak[8 * e + j]; sq += q8[j] * q8[j]; sk += k8[j] * k8[j]; }
            st16(sws, WS_KF + (((h0 + e) * 64 + tt) * 64 + pi + 32 * hh) * 16, u32x4{cvtpk(k8[0], k8[1]), cvtpk(k8[2], k8[3]), cvtpk(k8[4], k8[5]), cvtpk(k8[6], k8[7])});
            st16(sws, WS_QF + (((h0 + e) * 64 + tt) * 64 + i32 + 32 * hh) * 16, u32x4{cvtpk(q8[0], q8[1]), cvtpk(q8[2], q8[3]), cvtpk(q8[4], q8[5]), cvtpk(q8[6], q8[7])});
            sq += swap32(sq); sk += swap32(sk);
            sq = dppmax<0x142, 0xa>(rowmax16(sq)); sk = dppmax<0x142, 0xa>(rowmax16(sk));
            if (lane == 31) { LAS float* nr = (LAS float*)(lds + NRM_LDS) + (cf * 2 + tl) * 4; nr[e] = sk; nr[2 + e] = sq; }
        }
    }

    WAITV(0);
    WAITL0();
    if (lds_inc(lds, CNT_QKV) == 3 && lane < 32) {
        const int hl = lane >> 3;
        LAS const float* nr = (LAS const float*)(lds + NRM_LDS);
        const float vk = __builtin_fmaxf(nr[((hl >> 1) * 2 + 0) * 4 + (hl & 1)], nr[((hl >> 1) * 2 + 1) * 4 + (hl & 1)]);
        const float vq = __builtin_fmaxf(nr[((hl >> 1) * 2 + 0) * 4 + 2 + (hl & 1)], nr[((hl >> 1) * 2 + 1) * 4 + 2 + (hl & 1)]);
        put_flag(ws, WS_F1A + (((g4 * 4 + hl) * 8 + (lane & 7)) * 32 + rb) * 8, (tag << 24) | (code12(vk) << 12) | code12(vq));
        lds_add(lds, CNT_PUB);

    }
}

#define ATTN_LOOP_ASM \
  "buffer_load_dwordx4 v[120:123], %[qo], %[srd], 0 offen" SC1 "\n" \
  "buffer_load_dwordx4 v[124:127], %[qo], %[srd], 0 offen offset:1024" SC1 "\n" \
  "v_mov_b32 v116, 0xdddd\n" \
  "v_mov_b32 v100, %[fill]\n" \
  "v_mov_b32 v101, %[fill]\n" \
  "v_mov_b32 v102, %[fill]\n" \
  "v_mov_b32 v103, %[fill]\n" \
  "v_mov_b32 v104, %[fill]\n" \
  "v_mov_b32 v105, %[fill]\n" \
  "v_mov_b32 v106, %[fill]\n" \
  "v_mov_b32 v107, %[fill]\n" \
  "v_mov_b32 v108, %[fill]\n" \
  "v_mov_b32 v109, %[fill]\n" \
  "v_mov_b32 v110, %[fill]\n" \
  "v_mov_b32 v111, %[fill]\n" \
  "v_mov_b32 v112, %[fill]\n" \
  "v_mov_b32 v113, %[fill]\n" \
  "v_mov_b32 v114, %[fill]\n" \
  "v_mov_b32 v115, %[fill]\n" \
  "Sa%=:\n" \
  "ds_read_b32 v118, %[cnt] offset:0\n" \
  "s_waitcnt lgkmcnt(0)\n" \
  "v_readfirstlane_b32 %[t], v118\n" \
  "s_nop 3\n" \
  "s_cmp_ge_u32 %[t], 4\n" \
  "s_cbranch_scc1 Da%=\n" \
  "s_sleep 1\n" \
  "s_branch Sa%=\n" \
  "Da%=:\n" \
  "ds_read_b128 v[96:99], %[ka] offset:0\n" \
  "s_mov_b64 exec, %[vm]\n" \
  "ds_read_b128 v[100:103], %[va] offset:0\n" \
  "ds_read_b128 v[104:107], %[va] offset:512\n" \
  "s_mov_b64 exec, -1\n" \
  "s_waitcnt vmcnt(0) lgkmcnt(2)\n" \
  "v_mfma_f32_32x32x16_bf16 v[64:79], v[96:99], v[120:123], 0\n" \
  "v_mfma_f32_32x32x16_bf16 v[80:95], v[96:99], v[124:127], 0\n" \
  "s_nop 7\n" \
  "s_nop 3\n" \
  "v_exp_f32 v64, v64\n" \
  "v_exp_f32 v65, v65\n" \
  "v_exp_f32 v66, v66\n" \
  "v_exp_f32 v67, v67\n" \
  "v_exp_f32 v68, v68\n" \
  "s_mov_b32 %[g], 0\n" \
  "LOOP%=:\n" \
  "s_cmp_eq_u32 %[g], 1\n" \
  "s_cbranch_scc1 W1%=\n" \
  "s_cmp_eq_u32 %[g], 3\n" \
  "s_cbranch_scc1 W3%=\n" \
  "s_cmp_eq_u32 %[g], 5\n" \
  "s_cbranch_scc1 W5%=\n" \
  "BODY%=:\n" \
  "s_mov_b64 exec, %[vm]\n" \
  "ds_read_b128 v[108:111], %[va] offset:1024\n" \
  "ds_read_b128 v[112:115], %[va] offset:1536\n" \
  "s_mov_b64 exec, -1\n" \
  "ds_read_b128 v[96:99], %[ka] offset:1024\n" \
  "v_exp_f32 v69, v69\n" \
  "v_exp_f32 v70, v70\n" \
  "v_exp_f32 v71, v71\n" \
  "v_exp_f32 v72, v72\n" \
  "v_exp_f32 v73, v73\n" \
  "s_waitcnt lgkmcnt(3)\n" \
  "v_smfmac_f32_32x32x32_bf16 %[o1], v[100:103], v[64:71], v116\n" \
  "v_exp_f32 v74, v74\n" \
  "v_exp_f32 v75, v75\n" \
  "v_exp_f32 v76, v76\n" \
  "v_exp_f32 v77, v77\n" \
  "v_exp_f32 v78, v78\n" \
  "v_exp_f32 v79, v79\n" \
  "v_exp_f32 v80, v80\n" \
  "v_exp_f32 v81, v81\n" \
  "v_smfmac_f32_32x32x32_bf16 %[o1], v[104:107], v[72:79], v116\n" \
  "v_exp_f32 v82, v82\n" \
  "v_exp_f32 v83, v83\n" \
  "v_exp_f32 v84, v84\n" \
  "s_waitcnt lgkmcnt(0)\n" \
  "v_mfma_f32_32x32x16_bf16 v[64:79], v[96:99], v[120:123], 0\n" \
  "v_exp_f32 v85, v85\n" \
  "v_exp_f32 v86, v86\n" \
  "v_exp_f32 v87, v87\n" \
  "v_exp_f32 v88, v88\n" \
  "v_exp_f32 v89, v89\n" \
  "v_smfmac_f32_32x32x32_bf16 %[o2], v[100:103], v[80:87], v116\n" \
  "v_exp_f32 v90, v90\n" \
  "v_exp_f32 v91, v91\n" \
  "v_exp_f32 v92, v92\n" \
  "v_exp_f32 v93, v93\n" \
  "v_exp_f32 v94, v94\n" \
  "v_exp_f32 v95, v95\n" \
  "v_exp_f32 v64, v64\n" \
  "v_exp_f32 v65, v65\n" \
  "v_smfmac_f32_32x32x32_bf16 %[o2], v[104:107], v[88:95], v116\n" \
  "v_exp_f32 v66, v66\n" \
  "v_exp_f32 v67, v67\n" \
  "v_exp_f32 v68, v68\n" \
  "v_mfma_f32_32x32x16_bf16 v[80:95], v[96:99], v[124:127], 0\n" \
  "s_mov_b64 exec, %[vm]\n" \
  "ds_read_b128 v[100:103], %[va] offset:2048\n" \
  "ds_read_b128 v[104:107], %[va] offset:2560\n" \
  "s_mov_b64 exec, -1\n" \
  "ds_read_b128 v[96:99], %[ka] offset:2048\n" \
  "v_exp_f32 v69, v69\n" \
  "v_exp_f32 v70, v70\n" \
  "v_exp_f32 v71, v71\n" \
  "v_exp_f32 v72, v72\n" \
  "v_exp_f32 v73, v73\n" \
  "s_waitcnt lgkmcnt(3)\n" \
  "v_smfmac_f32_32x32x32_bf16 %[o1], v[108:111], v[64:71], v116\n" \
  "v_exp_f32 v74, v74\n" \
  "v_exp_f32 v75, v75\n" \
  "v_exp_f32 v76, v76\n" \
  "v_exp_f32 v77, v77\n" \
  "v_exp_f32 v78, v78\n" \
  "v_exp_f32 v79, v79\n" \
  "v_exp_f32 v80, v80\n" \
  "v_exp_f32 v81, v81\n" \
  "v_smfmac_f32_32x32x32_bf16 %[o1], v[112:115], v[72:79], v116\n" \
  "v_exp_f32 v82, v82\n" \
  "v_exp_f32 v83, v83\n" \
  "v_exp_f32 v84, v84\n" \
  "s_waitcnt lgkmcnt(0)\n" \
  "v_mfma_f32_32x32x16_bf16 v[64:79], v[96:99], v[120:123], 0\n" \
  "v_exp_f32 v85, v85\n" \
  "v_exp_f32 v86, v86\n" \
  "v_exp_f32 v87, v87\n" \
  "v_exp_f32 v88, v88\n" \
  "v_exp_f32 v89, v89\n" \
  "v_smfmac_f32_32x32x32_bf16 %[o2], v[108:111], v[80:87], v116\n" \
  "v_exp_f32 v90, v90\n" \
  "v_exp_f32 v91, v91\n" \
  "v_exp_f32 v92, v92\n" \
  "v_exp_f32 v93, v93\n" \
  "v_exp_f32 v94, v94\n" \
  "v_exp_f32 v95, v95\n" \
  "v_exp_f32 v64, v64\n" \
  "v_exp_f32 v65, v65\n" \
  "v_smfmac_f32_32x32x32_bf16 %[o2], v[112:115], v[88:95], v116\n" \
  "v_exp_f32 v66, v66\n" \
  "v_exp_f32 v67, v67\n" \
  "v_exp_f32 v68, v68\n" \
  "v_mfma_f32_32x32x16_bf16 v[80:95], v[96:99], v[124:127], 0\n" \
  "s_mov_b64 exec, %[vm]\n" \
  "ds_read_b128 v[108:111], %[va] offset:3072\n" \
  "ds_read_b128 v[112:115], %[va] offset:3584\n" \
  "s_mov_b64 exec, -1\n" \
  "ds_read_b128 v[96:99], %[ka] offset:3072\n" \
  "v_exp_f32 v69, v69\n" \
  "v_exp_f32 v70, v70\n" \
  "v_exp_f32 v71, v71\n" \
  "v_exp_f32 v72, v72\n" \
  "v_exp_f32 v73, v73\n" \
  "s_waitcnt lgkmcnt(3)\n" \
  "v_smfmac_f32_32x32x32_bf16 %[o1], v[100:103], v[64:71], v116\n" \
  "v_exp_f32 v74, v74\n" \
  "v_exp_f32 v75, v75\n" \
  "v_exp_f32 v76, v76\n" \
  "v_exp_f32 v77, v77\n" \
  "v_exp_f32 v78, v78\n" \
  "v_exp_f32 v79, v79\n" \
  "v_exp_f32 v80, v80\n" \
  "v_exp_f32 v81, v81\n" \
  "v_smfmac_f32_32x32x32_bf16 %[o1], v[104:107], v[72:79], v116\n" \
  "v_exp_f32 v82, v82\n" \
  "v_exp_f32 v83, v83\n" \
  "v_exp_f32 v84, v84\n" \
  "s_waitcnt lgkmcnt(0)\n" \
  "v_mfma_f32_32x32x16_bf16 v[64:79], v[96:99], v[120:123], 0\n" \
  "s_cmp_eq_u32 %[g], %[sw]\n" \
  "s_cbranch_scc0 NP%=\n" \
  "s_setprio 1\n" \
  "NP%=:\n" \
  "v_exp_f32 v85, v85\n" \
  "v_exp_f32 v86, v86\n" \
  "v_exp_f32 v87, v87\n" \
  "v_exp_f32 v88, v88\n" \
  "v_exp_f32 v89, v89\n" \
  "v_smfmac_f32_32x32x32_bf16 %[o2], v[100:103], v[80:87], v116\n" \
  "v_exp_f32 v90, v90\n" \
  "v_exp_f32 v91, v91\n" \
  "v_exp_f32 v92, v92\n" \
  "v_exp_f32 v93, v93\n" \
  "v_exp_f32 v94, v94\n" \
  "v_exp_f32 v95, v95\n" \
  "v_exp_f32 v64, v64\n" \
  "v_exp_f32 v65, v65\n" \
  "v_smfmac_f32_32x32x32_bf16 %[o2], v[104:107], v[88:95], v116\n" \
  "v_exp_f32 v66, v66\n" \
  "v_exp_f32 v67, v67\n" \
  "v_exp_f32 v68, v68\n" \
  "v_mfma_f32_32x32x16_bf16 v[80:95], v[96:99], v[124:127], 0\n" \
  "s_mov_b64 exec, %[vm]\n" \
  "ds_read_b128 v[100:103], %[va] offset:4096\n" \
  "ds_read_b128 v[104:107], %[va] offset:4608\n" \
  "s_mov_b64 exec, -1\n" \
  "ds_read_b128 v[96:99], %[ka] offset:4096\n" \
  "v_exp_f32 v69, v69\n" \
  "v_exp_f32 v70, v70\n" \
  "v_exp_f32 v71, v71\n" \
  "v_exp_f32 v72, v72\n" \
  "v_exp_f32 v73, v73\n" \
  "s_waitcnt lgkmcnt(3)\n" \
  "v_smfmac_f32_32x32x32_bf16 %[o1], v[108:111], v[64:71], v116\n" \
  "v_exp_f32 v74, v74\n" \
  "v_exp_f32 v75, v75\n" \
  "v_exp_f32 v76, v76\n" \
  "v_exp_f32 v77, v77\n" \
  "v_exp_f32 v78, v78\n" \
  "v_exp_f32 v79, v79\n" \
  "v_exp_f32 v80, v80\n" \
  "v_exp_f32 v81, v81\n" \
  "v_smfmac_f32_32x32x32_bf16 %[o1], v[112:115], v[72:79], v116\n" \
  "v_exp_f32 v82, v82\n" \
  "v_exp_f32 v83, v83\n" \
  "v_exp_f32 v84, v84\n" \
  "s_waitcnt lgkmcnt(0)\n" \
  "v_mfma_f32_32x32x16_bf16 v[64:79], v[96:99], v[120:123], 0\n" \
  "v_exp_f32 v85, v85\n" \
  "v_exp_f32 v86, v86\n" \
  "v_exp_f32 v87, v87\n" \
  "v_exp_f32 v88, v88\n" \
  "v_exp_f32 v89, v89\n" \
  "v_smfmac_f32_32x32x32_bf16 %[o2], v[108:111], v[80:87], v116\n" \
  "v_exp_f32 v90, v90\n" \
  "v_exp_f32 v91, v91\n" \
  "v_exp_f32 v92, v92\n" \
  "v_exp_f32 v93, v93\n" \
  "v_exp_f32 v94, v94\n" \
  "v_exp_f32 v95, v95\n" \
  "v_exp_f32 v64, v64\n" \
  "v_exp_f32 v65, v65\n" \
  "v_smfmac_f32_32x32x32_bf16 %[o2], v[112:115], v[88:95], v116\n" \
  "v_exp_f32 v66, v66\n" \
  "v_exp_f32 v67, v67\n" \
  "v_exp_f32 v68, v68\n" \
  "v_mfma_f32_32x32x16_bf16 v[80:95], v[96:99], v[124:127], 0\n" \
  "v_add_u32 %[ka], 4096, %[ka]\n" \
  "v_add_u32 %[va], 4096, %[va]\n" \
  "s_add_u32 %[g], %[g], 1\n" \
  "s_cmp_lt_u32 %[g], 8\n" \
  "s_cbranch_scc1 LOOP%=\n" \
  "s_branch END%=\n" \
  "W1%=:\n" \
  "Sb%=:\n" \
  "ds_read_b32 v118, %[cnt] offset:4\n" \
  "s_waitcnt lgkmcnt(0)\n" \
  "v_readfirstlane_b32 %[t], v118\n" \
  "s_nop 3\n" \
  "s_cmp_ge_u32 %[t], 4\n" \
  "s_cbranch_scc1 Db%=\n" \
  "s_sleep 1\n" \
  "s_branch Sb%=\n" \
  "Db%=:\n" \
  "s_branch BODY%=\n" \
  "W3%=:\n" \
  "Sc%=:\n" \
  "ds_read_b32 v118, %[cnt] offset:8\n" \
  "s_waitcnt lgkmcnt(0)\n" \
  "v_readfirstlane_b32 %[t], v118\n" \
  "s_nop 3\n" \
  "s_cmp_ge_u32 %[t], 4\n" \
  "s_cbranch_scc1 Dc%=\n" \
  "s_sleep 1\n" \
  "s_branch Sc%=\n" \
  "Dc%=:\n" \
  "s_branch BODY%=\n" \
  "W5%=:\n" \
  "Sd%=:\n" \
  "ds_read_b32 v118, %[cnt] offset:12\n" \
  "s_waitcnt lgkmcnt(0)\n" \
  "v_readfirstlane_b32 %[t], v118\n" \
  "s_nop 3\n" \
  "s_cmp_ge_u32 %[t], 4\n" \
  "s_cbranch_scc1 Dd%=\n" \
  "s_sleep 1\n" \
  "s_branch Sd%=\n" \
  "Dd%=:\n" \
  "s_branch BODY%=\n" \
  "END%=:\n" \
  "s_setprio 0\n" \
  "s_waitcnt lgkmcnt(0)\n" \
  "s_nop 15\n" \
  "s_nop 7\n"
#define ATTN_LOOP_CLOBBERS "v64", "v65", "v66", "v67", "v68", "v69", "v70", "v71", "v72", "v73", "v74", "v75", "v76", "v77", "v78", "v79", "v80", "v81", "v82", "v83", "v84", "v85", "v86", "v87", "v88", "v89", "v90", "v91", "v92", "v93", "v94", "v95", "v96", "v97", "v98", "v99", "v100", "v101", "v102", "v103", "v104", "v105", "v106", "v107", "v108", "v109", "v110", "v111", "v112", "v113", "v114", "v115", "v116", "v117", "v118", "v119", "v120", "v121", "v122", "v123", "v124", "v125", "v126", "v127"
#define KV_LOAD_ASM \
  "s_add_u32 m0, %[l0], 0\n" \
  "s_add_u32 %[t], %[kb], 0\n" \
  "buffer_load_dwordx4 %[vo], %[srd], %[t] offen" SC1 " lds\n" \
  "s_add_u32 m0, %[l0], 1024\n" \
  "s_add_u32 %[t], %[kb], 1024\n" \
  "buffer_load_dwordx4 %[vo], %[srd], %[t] offen" SC1 " lds\n" \
  "s_add_u32 m0, %[l0], 65536\n" \
  "s_add_u32 %[t], %[kb], 2097152\n" \
  "buffer_load_dwordx4 %[vo], %[srd], %[t] offen" SC1 " lds\n" \
  "s_add_u32 m0, %[l0], 66560\n" \
  "s_add_u32 %[t], %[kb], 2098176\n" \
  "buffer_load_dwordx4 %[vo], %[srd], %[t] offen" SC1 " lds\n" \
  "s_add_u32 m0, %[l0], 32768\n" \
  "s_add_u32 %[t], %[kb], 32768\n" \
  "buffer_load_dwordx4 %[vo], %[srd], %[t] offen" SC1 " lds\n" \
  "s_add_u32 m0, %[l0], 33792\n" \
  "s_add_u32 %[t], %[kb], 33792\n" \
  "buffer_load_dwordx4 %[vo], %[srd], %[t] offen" SC1 " lds\n" \
  "s_add_u32 m0, %[l0], 98304\n" \
  "s_add_u32 %[t], %[kb], 2129920\n" \
  "buffer_load_dwordx4 %[vo], %[srd], %[t] offen" SC1 " lds\n" \
  "s_add_u32 m0, %[l0], 99328\n" \
  "s_add_u32 %[t], %[kb], 2130944\n" \
  "buffer_load_dwordx4 %[vo], %[srd], %[t] offen" SC1 " lds\n" \
  "s_add_u32 m0, %[l0], 8192\n" \
  "s_add_u32 %[t], %[kb], 8192\n" \
  "buffer_load_dwordx4 %[vo], %[srd], %[t] offen" SC1 " lds\n" \
  "s_add_u32 m0, %[l0], 9216\n" \
  "s_add_u32 %[t], %[kb], 9216\n" \
  "buffer_load_dwordx4 %[vo], %[srd], %[t] offen" SC1 " lds\n" \
  "s_add_u32 m0, %[l0], 73728\n" \
  "s_add_u32 %[t], %[kb], 2105344\n" \
  "buffer_load_dwordx4 %[vo], %[srd], %[t] offen" SC1 " lds\n" \
  "s_add_u32 m0, %[l0], 74752\n" \
  "s_add_u32 %[t], %[kb], 2106368\n" \
  "buffer_load_dwordx4 %[vo], %[srd], %[t] offen" SC1 " lds\n" \
  "s_add_u32 m0, %[l0], 40960\n" \
  "s_add_u32 %[t], %[kb], 40960\n" \
  "buffer_load_dwordx4 %[vo], %[srd], %[t] offen" SC1 " lds\n" \
  "s_add_u32 m0, %[l0], 41984\n" \
  "s_add_u32 %[t], %[kb], 41984\n" \
  "buffer_load_dwordx4 %[vo], %[srd], %[t] offen" SC1 " lds\n" \
  "s_add_u32 m0, %[l0], 106496\n" \
  "s_add_u32 %[t], %[kb], 2138112\n" \
  "buffer_load_dwordx4 %[vo], %[srd], %[t] offen" SC1 " lds\n" \
  "s_add_u32 m0, %[l0], 107520\n" \
  "s_add_u32 %[t], %[kb], 2139136\n" \
  "buffer_load_dwordx4 %[vo], %[srd], %[t] offen" SC1 " lds\n" \
  "s_add_u32 m0, %[l0], 16384\n" \
  "s_add_u32 %[t], %[kb], 16384\n" \
  "buffer_load_dwordx4 %[vo], %[srd], %[t] offen" SC1 " lds\n" \
  "s_add_u32 m0, %[l0], 17408\n" \
  "s_add_u32 %[t], %[kb], 17408\n" \
  "buffer_load_dwordx4 %[vo], %[srd], %[t] offen" SC1 " lds\n" \
  "s_add_u32 m0, %[l0], 81920\n" \
  "s_add_u32 %[t], %[kb], 2113536\n" \
  "buffer_load_dwordx4 %[vo], %[srd], %[t] offen" SC1 " lds\n" \
  "s_add_u32 m0, %[l0], 82944\n" \
  "s_add_u32 %[t], %[kb], 2114560\n" \
  "buffer_load_dwordx4 %[vo], %[srd], %[t] offen" SC1 " lds\n" \
  "s_add_u32 m0, %[l0], 49152\n" \
  "s_add_u32 %[t], %[kb], 49152\n" \
  "buffer_load_dwordx4 %[vo], %[srd], %[t] offen" SC1 " lds\n" \
  "s_add_u32 m0, %[l0], 50176\n" \
  "s_add_u32 %[t], %[kb], 50176\n" \
  "buffer_load_dwordx4 %[vo], %[srd], %[t] offen" SC1 " lds\n" \
  "s_add_u32 m0, %[l0], 114688\n" \
  "s_add_u32 %[t], %[kb], 2146304\n" \
  "buffer_load_dwordx4 %[vo], %[srd], %[t] offen" SC1 " lds\n" \
  "s_add_u32 m0, %[l0], 115712\n" \
  "s_add_u32 %[t], %[kb], 2147328\n" \
  "buffer_load_dwordx4 %[vo], %[srd], %[t] offen" SC1 " lds\n" \
  "s_add_u32 m0, %[l0], 24576\n" \
  "s_add_u32 %[t], %[kb], 24576\n" \
  "buffer_load_dwordx4 %[vo], %[srd], %[t] offen" SC1 " lds\n" \
  "s_add_u32 m0, %[l0], 25600\n" \
  "s_add_u32 %[t], %[kb], 25600\n" \
  "buffer_load_dwordx4 %[vo], %[srd], %[t] offen" SC1 " lds\n" \
  "s_add_u32 m0, %[l0], 90112\n" \
  "s_add_u32 %[t], %[kb], 2121728\n" \
  "buffer_load_dwordx4 %[vo], %[srd], %[t] offen" SC1 " lds\n" \
  "s_add_u32 m0, %[l0], 91136\n" \
  "s_add_u32 %[t], %[kb], 2122752\n" \
  "buffer_load_dwordx4 %[vo], %[srd], %[t] offen" SC1 " lds\n" \
  "s_add_u32 m0, %[l0], 57344\n" \
  "s_add_u32 %[t], %[kb], 57344\n" \
  "buffer_load_dwordx4 %[vo], %[srd], %[t] offen" SC1 " lds\n" \
  "s_add_u32 m0, %[l0], 58368\n" \
  "s_add_u32 %[t], %[kb], 58368\n" \
  "buffer_load_dwordx4 %[vo], %[srd], %[t] offen" SC1 " lds\n" \
  "s_add_u32 m0, %[l0], 122880\n" \
  "s_add_u32 %[t], %[kb], 2154496\n" \
  "buffer_load_dwordx4 %[vo], %[srd], %[t] offen" SC1 " lds\n" \
  "s_add_u32 m0, %[l0], 123904\n" \
  "s_add_u32 %[t], %[kb], 2155520\n" \
  "buffer_load_dwordx4 %[vo], %[srd], %[t] offen" SC1 " lds\n"
#define CONV_LOOP_ASM \
  "ds_read2_b32 v[64:65], %[ap] offset0:0 offset1:1\n" \
  "ds_read2_b32 v[66:67], %[ap] offset0:2 offset1:3\n" \
  "ds_read_b128 v[76:79], %[bp] offset:0\n" \
  "ds_read_b128 v[72:75], %[bp] offset:2560\n" \
  "ds_read2_b32 v[68:69], %[aq] offset0:0 offset1:1\n" \
  "ds_read2_b32 v[70:71], %[aq] offset0:2 offset1:3\n" \
  "ds_read2_b32 v[80:81], %[ap] offset0:8 offset1:9\n" \
  "ds_read2_b32 v[82:83], %[ap] offset0:10 offset1:11\n" \
  "s_waitcnt lgkmcnt(2)\n" \
  "v_mfma_f32_32x32x16_bf16 %[a1], v[64:67], v[76:79], %[a1]\n" \
  "ds_read_b128 v[92:95], %[bp] offset:32\n" \
  "ds_read_b128 v[88:91], %[bp] offset:2592\n" \
  "v_mfma_f32_32x32x16_bf16 %[a0], v[64:67], v[72:75], %[a0]\n" \
  "ds_read2_b32 v[84:85], %[aq] offset0:8 offset1:9\n" \
  "ds_read2_b32 v[86:87], %[aq] offset0:10 offset1:11\n" \
  "v_mfma_f32_32x32x16_bf16 %[a1], v[68:71], v[72:75], %[a1]\n" \
  "ds_read2_b32 v[64:65], %[ap] offset0:16 offset1:17\n" \
  "ds_read2_b32 v[66:67], %[ap] offset0:18 offset1:19\n" \
  "s_waitcnt lgkmcnt(2)\n" \
  "v_mfma_f32_32x32x16_bf16 %[a1], v[80:83], v[92:95], %[a1]\n" \
  "ds_read_b128 v[76:79], %[bp] offset:80\n" \
  "ds_read_b128 v[72:75], %[bp] offset:2640\n" \
  "v_mfma_f32_32x32x16_bf16 %[a0], v[80:83], v[88:91], %[a0]\n" \
  "ds_read2_b32 v[68:69], %[aq] offset0:16 offset1:17\n" \
  "ds_read2_b32 v[70:71], %[aq] offset0:18 offset1:19\n" \
  "v_mfma_f32_32x32x16_bf16 %[a1], v[84:87], v[88:91], %[a1]\n" \
  "ds_read2_b32 v[80:81], %[ap] offset0:24 offset1:25\n" \
  "ds_read2_b32 v[82:83], %[ap] offset0:26 offset1:27\n" \
  "s_waitcnt lgkmcnt(2)\n" \
  "v_mfma_f32_32x32x16_bf16 %[a1], v[64:67], v[76:79], %[a1]\n" \
  "ds_read_b128 v[92:95], %[bp] offset:112\n" \
  "ds_read_b128 v[88:91], %[bp] offset:2672\n" \
  "v_mfma_f32_32x32x16_bf16 %[a0], v[64:67], v[72:75], %[a0]\n" \
  "ds_read2_b32 v[84:85], %[aq] offset0:24 offset1:25\n" \
  "ds_read2_b32 v[86:87], %[aq] offset0:26 offset1:27\n" \
  "v_mfma_f32_32x32x16_bf16 %[a1], v[68:71], v[72:75], %[a1]\n" \
  "ds_read2_b32 v[64:65], %[ap] offset0:32 offset1:33\n" \
  "ds_read2_b32 v[66:67], %[ap] offset0:34 offset1:35\n" \
  "s_waitcnt lgkmcnt(2)\n" \
  "v_mfma_f32_32x32x16_bf16 %[a1], v[80:83], v[92:95], %[a1]\n" \
  "ds_read_b128 v[76:79], %[bp] offset:160\n" \
  "ds_read_b128 v[72:75], %[bp] offset:2720\n" \
  "v_mfma_f32_32x32x16_bf16 %[a0], v[80:83], v[88:91], %[a0]\n" \
  "ds_read2_b32 v[68:69], %[aq] offset0:32 offset1:33\n" \
  "ds_read2_b32 v[70:71], %[aq] offset0:34 offset1:35\n" \
  "v_mfma_f32_32x32x16_bf16 %[a1], v[84:87], v[88:91], %[a1]\n" \
  "ds_read2_b32 v[80:81], %[ap] offset0:40 offset1:41\n" \
  "ds_read2_b32 v[82:83], %[ap] offset0:42 offset1:43\n" \
  "s_waitcnt lgkmcnt(2)\n" \
  "v_mfma_f32_32x32x16_bf16 %[a1], v[64:67], v[76:79], %[a1]\n" \
  "ds_read_b128 v[92:95], %[bp] offset:192\n" \
  "ds_read_b128 v[88:91], %[bp] offset:2752\n" \
  "v_mfma_f32_32x32x16_bf16 %[a0], v[64:67], v[72:75], %[a0]\n" \
  "ds_read2_b32 v[84:85], %[aq] offset0:40 offset1:41\n" \
  "ds_read2_b32 v[86:87], %[aq] offset0:42 offset1:43\n" \
  "v_mfma_f32_32x32x16_bf16 %[a1], v[68:71], v[72:75], %[a1]\n" \
  "ds_read2_b32 v[64:65], %[ap] offset0:48 offset1:49\n" \
  "ds_read2_b32 v[66:67], %[ap] offset0:50 offset1:51\n" \
  "s_waitcnt lgkmcnt(2)\n" \
  "v_mfma_f32_32x32x16_bf16 %[a1], v[80:83], v[92:95], %[a1]\n" \
  "ds_read_b128 v[76:79], %[bp] offset:240\n" \
  "ds_read_b128 v[72:75], %[bp] offset:2800\n" \
  "v_mfma_f32_32x32x16_bf16 %[a0], v[80:83], v[88:91], %[a0]\n" \
  "ds_read2_b32 v[68:69], %[aq] offset0:48 offset1:49\n" \
  "ds_read2_b32 v[70:71], %[aq] offset0:50 offset1:51\n" \
  "v_mfma_f32_32x32x16_bf16 %[a1], v[84:87], v[88:91], %[a1]\n" \
  "ds_read2_b32 v[80:81], %[ap] offset0:56 offset1:57\n" \
  "ds_read2_b32 v[82:83], %[ap] offset0:58 offset1:59\n" \
  "s_waitcnt lgkmcnt(2)\n" \
  "v_mfma_f32_32x32x16_bf16 %[a1], v[64:67], v[76:79], %[a1]\n" \
  "ds_read_b128 v[92:95], %[bp] offset:272\n" \
  "ds_read_b128 v[88:91], %[bp] offset:2832\n" \
  "v_mfma_f32_32x32x16_bf16 %[a0], v[64:67], v[72:75], %[a0]\n" \
  "ds_read2_b32 v[84:85], %[aq] offset0:56 offset1:57\n" \
  "ds_read2_b32 v[86:87], %[aq] offset0:58 offset1:59\n" \
  "v_mfma_f32_32x32x16_bf16 %[a1], v[68:71], v[72:75], %[a1]\n" \
  "ds_read2_b32 v[64:65], %[ap] offset0:64 offset1:65\n" \
  "ds_read2_b32 v[66:67], %[ap] offset0:66 offset1:67\n" \
  "s_waitcnt lgkmcnt(2)\n" \
  "v_mfma_f32_32x32x16_bf16 %[a1], v[80:83], v[92:95], %[a1]\n" \
  "ds_read_b128 v[76:79], %[bp] offset:320\n" \
  "ds_read_b128 v[72:75], %[bp] offset:2880\n" \
  "v_mfma_f32_32x32x16_bf16 %[a0], v[80:83], v[88:91], %[a0]\n" \
  "ds_read2_b32 v[68:69], %[aq] offset0:64 offset1:65\n" \
  "ds_read2_b32 v[70:71], %[aq] offset0:66 offset1:67\n" \
  "v_mfma_f32_32x32x16_bf16 %[a1], v[84:87], v[88:91], %[a1]\n" \
  "ds_read2_b32 v[80:81], %[ap] offset0:72 offset1:73\n" \
  "ds_read2_b32 v[82:83], %[ap] offset0:74 offset1:75\n" \
  "s_waitcnt lgkmcnt(2)\n" \
  "v_mfma_f32_32x32x16_bf16 %[a1], v[64:67], v[76:79], %[a1]\n" \
  "ds_read_b128 v[92:95], %[bp] offset:352\n" \
  "ds_read_b128 v[88:91], %[bp] offset:2912\n" \
  "v_mfma_f32_32x32x16_bf16 %[a0], v[64:67], v[72:75], %[a0]\n" \
  "ds_read2_b32 v[84:85], %[aq] offset0:72 offset1:73\n" \
  "ds_read2_b32 v[86:87], %[aq] offset0:74 offset1:75\n" \
  "v_mfma_f32_32x32x16_bf16 %[a1], v[68:71], v[72:75], %[a1]\n" \
  "ds_read2_b32 v[64:65], %[ap] offset0:80 offset1:81\n" \
  "ds_read2_b32 v[66:67], %[ap] offset0:82 offset1:83\n" \
  "s_waitcnt lgkmcnt(2)\n" \
  "v_mfma_f32_32x32x16_bf16 %[a1], v[80:83], v[92:95], %[a1]\n" \
  "ds_read_b128 v[76:79], %[bp] offset:400\n" \
  "ds_read_b128 v[72:75], %[bp] offset:2960\n" \
  "v_mfma_f32_32x32x16_bf16 %[a0], v[80:83], v[88:91], %[a0]\n" \
  "ds_read2_b32 v[68:69], %[aq] offset0:80 offset1:81\n" \
  "ds_read2_b32 v[70:71], %[aq] offset0:82 offset1:83\n" \
  "v_mfma_f32_32x32x16_bf16 %[a1], v[84:87], v[88:91], %[a1]\n" \
  "ds_read2_b32 v[80:81], %[ap] offset0:88 offset1:89\n" \
  "ds_read2_b32 v[82:83], %[ap] offset0:90 offset1:91\n" \
  "s_waitcnt lgkmcnt(2)\n" \
  "v_mfma_f32_32x32x16_bf16 %[a1], v[64:67], v[76:79], %[a1]\n" \
  "ds_read_b128 v[92:95], %[bp] offset:432\n" \
  "ds_read_b128 v[88:91], %[bp] offset:2992\n" \
  "v_mfma_f32_32x32x16_bf16 %[a0], v[64:67], v[72:75], %[a0]\n" \
  "ds_read2_b32 v[84:85], %[aq] offset0:88 offset1:89\n" \
  "ds_read2_b32 v[86:87], %[aq] offset0:90 offset1:91\n" \
  "v_mfma_f32_32x32x16_bf16 %[a1], v[68:71], v[72:75], %[a1]\n" \
  "ds_read2_b32 v[64:65], %[ap] offset0:96 offset1:97\n" \
  "ds_read2_b32 v[66:67], %[ap] offset0:98 offset1:99\n" \
  "s_waitcnt lgkmcnt(2)\n" \
  "v_mfma_f32_32x32x16_bf16 %[a1], v[80:83], v[92:95], %[a1]\n" \
  "ds_read_b128 v[76:79], %[bp] offset:480\n" \
  "ds_read_b128 v[72:75], %[bp] offset:3040\n" \
  "v_mfma_f32_32x32x16_bf16 %[a0], v[80:83], v[88:91], %[a0]\n" \
  "ds_read2_b32 v[68:69], %[aq] offset0:96 offset1:97\n" \
  "ds_read2_b32 v[70:71], %[aq] offset0:98 offset1:99\n" \
  "v_mfma_f32_32x32x16_bf16 %[a1], v[84:87], v[88:91], %[a1]\n" \
  "ds_read2_b32 v[80:81], %[ap] offset0:104 offset1:105\n" \
  "ds_read2_b32 v[82:83], %[ap] offset0:106 offset1:107\n" \
  "s_waitcnt lgkmcnt(2)\n" \
  "v_mfma_f32_32x32x16_bf16 %[a1], v[64:67], v[76:79], %[a1]\n" \
  "ds_read_b128 v[92:95], %[bp] offset:512\n" \
  "ds_read_b128 v[88:91], %[bp] offset:3072\n" \
  "v_mfma_f32_32x32x16_bf16 %[a0], v[64:67], v[72:75], %[a0]\n" \
  "ds_read2_b32 v[84:85], %[aq] offset0:104 offset1:105\n" \
  "ds_read2_b32 v[86:87], %[aq] offset0:106 offset1:107\n" \
  "v_mfma_f32_32x32x16_bf16 %[a1], v[68:71], v[72:75], %[a1]\n" \
  "ds_read2_b32 v[64:65], %[ap] offset0:112 offset1:113\n" \
  "ds_read2_b32 v[66:67], %[ap] offset0:114 offset1:115\n" \
  "s_waitcnt lgkmcnt(2)\n" \
  "v_mfma_f32_32x32x16_bf16 %[a1], v[80:83], v[92:95], %[a1]\n" \
  "ds_read_b128 v[76:79], %[bp] offset:560\n" \
  "ds_read_b128 v[72:75], %[bp] offset:3120\n" \
  "v_mfma_f32_32x32x16_bf16 %[a0], v[80:83], v[88:91], %[a0]\n" \
  "ds_read2_b32 v[68:69], %[aq] offset0:112 offset1:113\n" \
  "ds_read2_b32 v[70:71], %[aq] offset0:114 offset1:115\n" \
  "v_mfma_f32_32x32x16_bf16 %[a1], v[84:87], v[88:91], %[a1]\n" \
  "ds_read2_b32 v[80:81], %[ap] offset0:120 offset1:121\n" \
  "ds_read2_b32 v[82:83], %[ap] offset0:122 offset1:123\n" \
  "s_waitcnt lgkmcnt(2)\n" \
  "v_mfma_f32_32x32x16_bf16 %[a1], v[64:67], v[76:79], %[a1]\n" \
  "ds_read_b128 v[92:95], %[bp] offset:592\n" \
  "ds_read_b128 v[88:91], %[bp] offset:3152\n" \
  "v_mfma_f32_32x32x16_bf16 %[a0], v[64:67], v[72:75], %[a0]\n" \
  "ds_read2_b32 v[84:85], %[aq] offset0:120 offset1:121\n" \
  "ds_read2_b32 v[86:87], %[aq] offset0:122 offset1:123\n" \
  "v_mfma_f32_32x32x16_bf16 %[a1], v[68:71], v[72:75], %[a1]\n" \
  "ds_read2_b32 v[64:65], %[ap] offset0:128 offset1:129\n" \
  "ds_read2_b32 v[66:67], %[ap] offset0:130 offset1:131\n" \
  "s_waitcnt lgkmcnt(2)\n" \
  "v_mfma_f32_32x32x16_bf16 %[a1], v[80:83], v[92:95], %[a1]\n" \
  "ds_read_b128 v[76:79], %[bp] offset:640\n" \
  "ds_read_b128 v[72:75], %[bp] offset:3200\n" \
  "v_mfma_f32_32x32x16_bf16 %[a0], v[80:83], v[88:91], %[a0]\n" \
  "ds_read2_b32 v[68:69], %[aq] offset0:128 offset1:129\n" \
  "ds_read2_b32 v[70:71], %[aq] offset0:130 offset1:131\n" \
  "v_mfma_f32_32x32x16_bf16 %[a1], v[84:87], v[88:91], %[a1]\n" \
  "ds_read2_b32 v[80:81], %[ap] offset0:136 offset1:137\n" \
  "ds_read2_b32 v[82:83], %[ap] offset0:138 offset1:139\n" \
  "s_waitcnt lgkmcnt(2)\n" \
  "v_mfma_f32_32x32x16_bf16 %[a1], v[64:67], v[76:79], %[a1]\n" \
  "ds_read_b128 v[92:95], %[bp] offset:672\n" \
  "ds_read_b128 v[88:91], %[bp] offset:3232\n" \
  "v_mfma_f32_32x32x16_bf16 %[a0], v[64:67], v[72:75], %[a0]\n" \
  "ds_read2_b32 v[84:85], %[aq] offset0:136 offset1:137\n" \
  "ds_read2_b32 v[86:87], %[aq] offset0:138 offset1:139\n" \
  "v_mfma_f32_32x32x16_bf16 %[a1], v[68:71], v[72:75], %[a1]\n" \
  "ds_read2_b32 v[64:65], %[ap] offset0:144 offset1:145\n" \
  "ds_read2_b32 v[66:67], %[ap] offset0:146 offset1:147\n" \
  "s_waitcnt lgkmcnt(2)\n" \
  "v_mfma_f32_32x32x16_bf16 %[a1], v[80:83], v[92:95], %[a1]\n" \
  "ds_read_b128 v[76:79], %[bp] offset:720\n" \
  "ds_read_b128 v[72:75], %[bp] offset:3280\n" \
  "v_mfma_f32_32x32x16_bf16 %[a0], v[80:83], v[88:91], %[a0]\n" \
  "ds_read2_b32 v[68:69], %[aq] offset0:144 offset1:145\n" \
  "ds_read2_b32 v[70:71], %[aq] offset0:146 offset1:147\n" \
  "v_mfma_f32_32x32x16_bf16 %[a1], v[84:87], v[88:91], %[a1]\n" \
  "ds_read2_b32 v[80:81], %[ap] offset0:152 offset1:153\n" \
  "ds_read2_b32 v[82:83], %[ap] offset0:154 offset1:155\n" \
  "s_waitcnt lgkmcnt(2)\n" \
  "v_mfma_f32_32x32x16_bf16 %[a1], v[64:67], v[76:79], %[a1]\n" \
  "ds_read_b128 v[92:95], %[bp] offset:752\n" \
  "ds_read_b128 v[88:91], %[bp] offset:3312\n" \
  "v_mfma_f32_32x32x16_bf16 %[a0], v[64:67], v[72:75], %[a0]\n" \
  "ds_read2_b32 v[84:85], %[aq] offset0:152 offset1:153\n" \
  "ds_read2_b32 v[86:87], %[aq] offset0:154 offset1:155\n" \
  "v_mfma_f32_32x32x16_bf16 %[a1], v[68:71], v[72:75], %[a1]\n" \
  "ds_read2_b32 v[64:65], %[ap] offset0:160 offset1:161\n" \
  "ds_read2_b32 v[66:67], %[ap] offset0:162 offset1:163\n" \
  "s_waitcnt lgkmcnt(2)\n" \
  "v_mfma_f32_32x32x16_bf16 %[a1], v[80:83], v[92:95], %[a1]\n" \
  "ds_read_b128 v[76:79], %[bp] offset:800\n" \
  "ds_read_b128 v[72:75], %[bp] offset:3360\n" \
  "v_mfma_f32_32x32x16_bf16 %[a0], v[80:83], v[88:91], %[a0]\n" \
  "ds_read2_b32 v[68:69], %[aq] offset0:160 offset1:161\n" \
  "ds_read2_b32 v[70:71], %[aq] offset0:162 offset1:163\n" \
  "v_mfma_f32_32x32x16_bf16 %[a1], v[84:87], v[88:91], %[a1]\n" \
  "ds_read2_b32 v[80:81], %[ap] offset0:168 offset1:169\n" \
  "ds_read2_b32 v[82:83], %[ap] offset0:170 offset1:171\n" \
  "s_waitcnt lgkmcnt(2)\n" \
  "v_mfma_f32_32x32x16_bf16 %[a1], v[64:67], v[76:79], %[a1]\n" \
  "ds_read_b128 v[92:95], %[bp] offset:832\n" \
  "ds_read_b128 v[88:91], %[bp] offset:3392\n" \
  "v_mfma_f32_32x32x16_bf16 %[a0], v[64:67], v[72:75], %[a0]\n" \
  "ds_read2_b32 v[84:85], %[aq] offset0:168 offset1:169\n" \
  "ds_read2_b32 v[86:87], %[aq] offset0:170 offset1:171\n" \
  "v_mfma_f32_32x32x16_bf16 %[a1], v[68:71], v[72:75], %[a1]\n" \
  "ds_read2_b32 v[64:65], %[ap] offset0:176 offset1:177\n" \
  "ds_read2_b32 v[66:67], %[ap] offset0:178 offset1:179\n" \
  "s_waitcnt lgkmcnt(2)\n" \
  "v_mfma_f32_32x32x16_bf16 %[a1], v[80:83], v[92:95], %[a1]\n" \
  "ds_read_b128 v[76:79], %[bp] offset:880\n" \
  "ds_read_b128 v[72:75], %[bp] offset:3440\n" \
  "v_mfma_f32_32x32x16_bf16 %[a0], v[80:83], v[88:91], %[a0]\n" \
  "ds_read2_b32 v[68:69], %[aq] offset0:176 offset1:177\n" \
  "ds_read2_b32 v[70:71], %[aq] offset0:178 offset1:179\n" \
  "v_mfma_f32_32x32x16_bf16 %[a1], v[84:87], v[88:91], %[a1]\n" \
  "ds_read2_b32 v[80:81], %[ap] offset0:184 offset1:185\n" \
  "ds_read2_b32 v[82:83], %[ap] offset0:186 offset1:187\n" \
  "s_waitcnt lgkmcnt(2)\n" \
  "v_mfma_f32_32x32x16_bf16 %[a1], v[64:67], v[76:79], %[a1]\n" \
  "ds_read_b128 v[92:95], %[bp] offset:912\n" \
  "ds_read_b128 v[88:91], %[bp] offset:3472\n" \
  "v_mfma_f32_32x32x16_bf16 %[a0], v[64:67], v[72:75], %[a0]\n" \
  "ds_read2_b32 v[84:85], %[aq] offset0:184 offset1:185\n" \
  "ds_read2_b32 v[86:87], %[aq] offset0:186 offset1:187\n" \
  "v_mfma_f32_32x32x16_bf16 %[a1], v[68:71], v[72:75], %[a1]\n" \
  "ds_read2_b32 v[64:65], %[ap] offset0:192 offset1:193\n" \
  "ds_read2_b32 v[66:67], %[ap] offset0:194 offset1:195\n" \
  "s_waitcnt lgkmcnt(2)\n" \
  "v_mfma_f32_32x32x16_bf16 %[a1], v[80:83], v[92:95], %[a1]\n" \
  "ds_read_b128 v[76:79], %[bp] offset:960\n" \
  "ds_read_b128 v[72:75], %[bp] offset:3520\n" \
  "v_mfma_f32_32x32x16_bf16 %[a0], v[80:83], v[88:91], %[a0]\n" \
  "ds_read2_b32 v[68:69], %[aq] offset0:192 offset1:193\n" \
  "ds_read2_b32 v[70:71], %[aq] offset0:194 offset1:195\n" \
  "v_mfma_f32_32x32x16_bf16 %[a1], v[84:87], v[88:91], %[a1]\n" \
  "ds_read2_b32 v[80:81], %[ap] offset0:200 offset1:201\n" \
  "ds_read2_b32 v[82:83], %[ap] offset0:202 offset1:203\n" \
  "s_waitcnt lgkmcnt(2)\n" \
  "v_mfma_f32_32x32x16_bf16 %[a1], v[64:67], v[76:79], %[a1]\n" \
  "ds_read_b128 v[92:95], %[bp] offset:992\n" \
  "ds_read_b128 v[88:91], %[bp] offset:3552\n" \
  "v_mfma_f32_32x32x16_bf16 %[a0], v[64:67], v[72:75], %[a0]\n" \
  "ds_read2_b32 v[84:85], %[aq] offset0:200 offset1:201\n" \
  "ds_read2_b32 v[86:87], %[aq] offset0:202 offset1:203\n" \
  "v_mfma_f32_32x32x16_bf16 %[a1], v[68:71], v[72:75], %[a1]\n" \
  "ds_read2_b32 v[64:65], %[ap] offset0:208 offset1:209\n" \
  "ds_read2_b32 v[66:67], %[ap] offset0:210 offset1:211\n" \
  "s_waitcnt lgkmcnt(2)\n" \
  "v_mfma_f32_32x32x16_bf16 %[a1], v[80:83], v[92:95], %[a1]\n" \
  "ds_read_b128 v[76:79], %[bp] offset:1040\n" \
  "ds_read_b128 v[72:75], %[bp] offset:3600\n" \
  "v_mfma_f32_32x32x16_bf16 %[a0], v[80:83], v[88:91], %[a0]\n" \
  "ds_read2_b32 v[68:69], %[aq] offset0:208 offset1:209\n" \
  "ds_read2_b32 v[70:71], %[aq] offset0:210 offset1:211\n" \
  "v_mfma_f32_32x32x16_bf16 %[a1], v[84:87], v[88:91], %[a1]\n" \
  "ds_read2_b32 v[80:81], %[ap] offset0:216 offset1:217\n" \
  "ds_read2_b32 v[82:83], %[ap] offset0:218 offset1:219\n" \
  "s_waitcnt lgkmcnt(2)\n" \
  "v_mfma_f32_32x32x16_bf16 %[a1], v[64:67], v[76:79], %[a1]\n" \
  "ds_read_b128 v[92:95], %[bp] offset:1072\n" \
  "ds_read_b128 v[88:91], %[bp] offset:3632\n" \
  "v_mfma_f32_32x32x16_bf16 %[a0], v[64:67], v[72:75], %[a0]\n" \
  "ds_read2_b32 v[84:85], %[aq] offset0:216 offset1:217\n" \
  "ds_read2_b32 v[86:87], %[aq] offset0:218 offset1:219\n" \
  "v_mfma_f32_32x32x16_bf16 %[a1], v[68:71], v[72:75], %[a1]\n" \
  "ds_read2_b32 v[64:65], %[ap] offset0:224 offset1:225\n" \
  "ds_read2_b32 v[66:67], %[ap] offset0:226 offset1:227\n" \
  "s_waitcnt lgkmcnt(2)\n" \
  "v_mfma_f32_32x32x16_bf16 %[a1], v[80:83], v[92:95], %[a1]\n" \
  "ds_read_b128 v[76:79], %[bp] offset:1120\n" \
  "ds_read_b128 v[72:75], %[bp] offset:3680\n" \
  "v_mfma_f32_32x32x16_bf16 %[a0], v[80:83], v[88:91], %[a0]\n" \
  "ds_read2_b32 v[68:69], %[aq] offset0:224 offset1:225\n" \
  "ds_read2_b32 v[70:71], %[aq] offset0:226 offset1:227\n" \
  "v_mfma_f32_32x32x16_bf16 %[a1], v[84:87], v[88:91], %[a1]\n" \
  "ds_read2_b32 v[80:81], %[ap] offset0:232 offset1:233\n" \
  "ds_read2_b32 v[82:83], %[ap] offset0:234 offset1:235\n" \
  "s_waitcnt lgkmcnt(2)\n" \
  "v_mfma_f32_32x32x16_bf16 %[a1], v[64:67], v[76:79], %[a1]\n" \
  "ds_read_b128 v[92:95], %[bp] offset:1152\n" \
  "ds_read_b128 v[88:91], %[bp] offset:3712\n" \
  "v_mfma_f32_32x32x16_bf16 %[a0], v[64:67], v[72:75], %[a0]\n" \
  "ds_read2_b32 v[84:85], %[aq] offset0:232 offset1:233\n" \
  "ds_read2_b32 v[86:87], %[aq] offset0:234 offset1:235\n" \
  "v_mfma_f32_32x32x16_bf16 %[a1], v[68:71], v[72:75], %[a1]\n" \
  "ds_read2_b32 v[64:65], %[ap] offset0:240 offset1:241\n" \
  "ds_read2_b32 v[66:67], %[ap] offset0:242 offset1:243\n" \
  "s_waitcnt lgkmcnt(2)\n" \
  "v_mfma_f32_32x32x16_bf16 %[a1], v[80:83], v[92:95], %[a1]\n" \
  "ds_read_b128 v[76:79], %[bp] offset:1200\n" \
  "ds_read_b128 v[72:75], %[bp] offset:3760\n" \
  "v_mfma_f32_32x32x16_bf16 %[a0], v[80:83], v[88:91], %[a0]\n" \
  "ds_read2_b32 v[68:69], %[aq] offset0:240 offset1:241\n" \
  "ds_read2_b32 v[70:71], %[aq] offset0:242 offset1:243\n" \
  "v_mfma_f32_32x32x16_bf16 %[a1], v[84:87], v[88:91], %[a1]\n" \
  "ds_read2_b32 v[80:81], %[ap] offset0:248 offset1:249\n" \
  "ds_read2_b32 v[82:83], %[ap] offset0:250 offset1:251\n" \
  "s_waitcnt lgkmcnt(2)\n" \
  "v_mfma_f32_32x32x16_bf16 %[a1], v[64:67], v[76:79], %[a1]\n" \
  "ds_read_b128 v[92:95], %[bp] offset:1232\n" \
  "ds_read_b128 v[88:91], %[bp] offset:3792\n" \
  "v_mfma_f32_32x32x16_bf16 %[a0], v[64:67], v[72:75], %[a0]\n" \
  "ds_read2_b32 v[84:85], %[aq] offset0:248 offset1:249\n" \
  "ds_read2_b32 v[86:87], %[aq] offset0:250 offset1:251\n" \
  "v_mfma_f32_32x32x16_bf16 %[a1], v[68:71], v[72:75], %[a1]\n" \
  "s_waitcnt lgkmcnt(0)\n" \
  "v_mfma_f32_32x32x16_bf16 %[a1], v[80:83], v[92:95], %[a1]\n" \
  "v_mfma_f32_32x32x16_bf16 %[a0], v[80:83], v[88:91], %[a0]\n" \
  "v_mfma_f32_32x32x16_bf16 %[a1], v[84:87], v[88:91], %[a1]\n" \
  "s_nop 15\n" \
  "s_nop 7\n"
#define CONV_LOOP_CLOBBERS "v64", "v65", "v66", "v67", "v68", "v69", "v70", "v71", "v72", "v73", "v74", "v75", "v76", "v77", "v78", "v79", "v80", "v81", "v82", "v83", "v84", "v85", "v86", "v87", "v88", "v89", "v90", "v91", "v92", "v93", "v94", "v95"
#define OUT_LOOP_ASM \
  "ds_read_b64_tr_b16 v[64:65], %[wp] offset:0\n" \
  "ds_read_b64_tr_b16 v[66:67], %[wp] offset:1024\n" \
  "ds_read_b128 v[68:71], %[oa] offset:0\n" \
  "ds_read_b64_tr_b16 v[72:73], %[wp] offset:32768\n" \
  "ds_read_b64_tr_b16 v[74:75], %[wp] offset:33792\n" \
  "ds_read_b128 v[76:79], %[ob] offset:0\n" \
  "ds_read_b64_tr_b16 v[80:81], %[wp] offset:2048\n" \
  "ds_read_b64_tr_b16 v[82:83], %[wp] offset:3072\n" \
  "ds_read_b128 v[84:87], %[oa] offset:1024\n" \
  "ds_read_b64_tr_b16 v[88:89], %[wp] offset:34816\n" \
  "ds_read_b64_tr_b16 v[90:91], %[wp] offset:35840\n" \
  "ds_read_b128 v[92:95], %[ob] offset:1024\n" \
  "s_waitcnt lgkmcnt(9)\n" \
  "v_mfma_f32_32x32x16_bf16 %[a0], v[64:67], v[68:71], %[a0]\n" \
  "ds_read_b64_tr_b16 v[64:65], %[wp] offset:4096\n" \
  "ds_read_b64_tr_b16 v[66:67], %[wp] offset:5120\n" \
  "ds_read_b128 v[68:71], %[oa] offset:2048\n" \
  "s_waitcnt lgkmcnt(9)\n" \
  "v_mfma_f32_32x32x16_bf16 %[a1], v[72:75], v[76:79], %[a1]\n" \
  "ds_read_b64_tr_b16 v[72:73], %[wp] offset:36864\n" \
  "ds_read_b64_tr_b16 v[74:75], %[wp] offset:37888\n" \
  "ds_read_b128 v[76:79], %[ob] offset:2048\n" \
  "s_waitcnt lgkmcnt(9)\n" \
  "v_mfma_f32_32x32x16_bf16 %[a0], v[80:83], v[84:87], %[a0]\n" \
  "ds_read_b64_tr_b16 v[80:81], %[wp] offset:6144\n" \
  "ds_read_b64_tr_b16 v[82:83], %[wp] offset:7168\n" \
  "ds_read_b128 v[84:87], %[oa] offset:3072\n" \
  "s_waitcnt lgkmcnt(9)\n" \
  "v_mfma_f32_32x32x16_bf16 %[a1], v[88:91], v[92:95], %[a1]\n" \
  "ds_read_b64_tr_b16 v[88:89], %[wp] offset:38912\n" \
  "ds_read_b64_tr_b16 v[90:91], %[wp] offset:39936\n" \
  "ds_read_b128 v[92:95], %[ob] offset:3072\n" \
  "s_waitcnt lgkmcnt(9)\n" \
  "v_mfma_f32_32x32x16_bf16 %[a0], v[64:67], v[68:71], %[a0]\n" \
  "ds_read_b64_tr_b16 v[64:65], %[wp] offset:8192\n" \
  "ds_read_b64_tr_b16 v[66:67], %[wp] offset:9216\n" \
  "ds_read_b128 v[68:71], %[oa] offset:4096\n" \
  "s_waitcnt lgkmcnt(9)\n" \
  "v_mfma_f32_32x32x16_bf16 %[a1], v[72:75], v[76:79], %[a1]\n" \
  "ds_read_b64_tr_b16 v[72:73], %[wp] offset:40960\n" \
  "ds_read_b64_tr_b16 v[74:75], %[wp] offset:41984\n" \
  "ds_read_b128 v[76:79], %[ob] offset:4096\n" \
  "s_waitcnt lgkmcnt(9)\n" \
  "v_mfma_f32_32x32x16_bf16 %[a0], v[80:83], v[84:87], %[a0]\n" \
  "ds_read_b64_tr_b16 v[80:81], %[wp] offset:10240\n" \
  "ds_read_b64_tr_b16 v[82:83], %[wp] offset:11264\n" \
  "ds_read_b128 v[84:87], %[oa] offset:5120\n" \
  "s_waitcnt lgkmcnt(9)\n" \
  "v_mfma_f32_32x32x16_bf16 %[a1], v[88:91], v[92:95], %[a1]\n" \
  "ds_read_b64_tr_b16 v[88:89], %[wp] offset:43008\n" \
  "ds_read_b64_tr_b16 v[90:91], %[wp] offset:44032\n" \
  "ds_read_b128 v[92:95], %[ob] offset:5120\n" \
  "s_waitcnt lgkmcnt(9)\n" \
  "v_mfma_f32_32x32x16_bf16 %[a0], v[64:67], v[68:71], %[a0]\n" \
  "ds_read_b64_tr_b16 v[64:65], %[wp] offset:12288\n" \
  "ds_read_b64_tr_b16 v[66:67], %[wp] offset:13312\n" \
  "ds_read_b128 v[68:71], %[oa] offset:6144\n" \
  "s_waitcnt lgkmcnt(9)\n" \
  "v_mfma_f32_32x32x16_bf16 %[a1], v[72:75], v[76:79], %[a1]\n" \
  "ds_read_b64_tr_b16 v[72:73], %[wp] offset:45056\n" \
  "ds_read_b64_tr_b16 v[74:75], %[wp] offset:46080\n" \
  "ds_read_b128 v[76:79], %[ob] offset:6144\n" \
  "s_waitcnt lgkmcnt(9)\n" \
  "v_mfma_f32_32x32x16_bf16 %[a0], v[80:83], v[84:87], %[a0]\n" \
  "ds_read_b64_tr_b16 v[80:81], %[wp] offset:14336\n" \
  "ds_read_b64_tr_b16 v[82:83], %[wp] offset:15360\n" \
  "ds_read_b128 v[84:87], %[oa] offset:7168\n" \
  "s_waitcnt lgkmcnt(9)\n" \
  "v_mfma_f32_32x32x16_bf16 %[a1], v[88:91], v[92:95], %[a1]\n" \
  "ds_read_b64_tr_b16 v[88:89], %[wp] offset:47104\n" \
  "ds_read_b64_tr_b16 v[90:91], %[wp] offset:48128\n" \
  "ds_read_b128 v[92:95], %[ob] offset:7168\n" \
  "s_waitcnt lgkmcnt(9)\n" \
  "v_mfma_f32_32x32x16_bf16 %[a0], v[64:67], v[68:71], %[a0]\n" \
  "ds_read_b64_tr_b16 v[64:65], %[wp] offset:16384\n" \
  "ds_read_b64_tr_b16 v[66:67], %[wp] offset:17408\n" \
  "ds_read_b128 v[68:71], %[oa] offset:8192\n" \
  "s_waitcnt lgkmcnt(9)\n" \
  "v_mfma_f32_32x32x16_bf16 %[a1], v[72:75], v[76:79], %[a1]\n" \
  "ds_read_b64_tr_b16 v[72:73], %[wp] offset:49152\n" \
  "ds_read_b64_tr_b16 v[74:75], %[wp] offset:50176\n" \
  "ds_read_b128 v[76:79], %[ob] offset:8192\n" \
  "s_waitcnt lgkmcnt(9)\n" \
  "v_mfma_f32_32x32x16_bf16 %[a0], v[80:83], v[84:87], %[a0]\n" \
  "ds_read_b64_tr_b16 v[80:81], %[wp] offset:18432\n" \
  "ds_read_b64_tr_b16 v[82:83], %[wp] offset:19456\n" \
  "ds_read_b128 v[84:87], %[oa] offset:9216\n" \
  "s_waitcnt lgkmcnt(9)\n" \
  "v_mfma_f32_32x32x16_bf16 %[a1], v[88:91], v[92:95], %[a1]\n" \
  "ds_read_b64_tr_b16 v[88:89], %[wp] offset:51200\n" \
  "ds_read_b64_tr_b16 v[90:91], %[wp] offset:52224\n" \
  "ds_read_b128 v[92:95], %[ob] offset:9216\n" \
  "s_waitcnt lgkmcnt(9)\n" \
  "v_mfma_f32_32x32x16_bf16 %[a0], v[64:67], v[68:71], %[a0]\n" \
  "ds_read_b64_tr_b16 v[64:65], %[wp] offset:20480\n" \
  "ds_read_b64_tr_b16 v[66:67], %[wp] offset:21504\n" \
  "ds_read_b128 v[68:71], %[oa] offset:10240\n" \
  "s_waitcnt lgkmcnt(9)\n" \
  "v_mfma_f32_32x32x16_bf16 %[a1], v[72:75], v[76:79], %[a1]\n" \
  "ds_read_b64_tr_b16 v[72:73], %[wp] offset:53248\n" \
  "ds_read_b64_tr_b16 v[74:75], %[wp] offset:54272\n" \
  "ds_read_b128 v[76:79], %[ob] offset:10240\n" \
  "s_waitcnt lgkmcnt(9)\n" \
  "v_mfma_f32_32x32x16_bf16 %[a0], v[80:83], v[84:87], %[a0]\n" \
  "ds_read_b64_tr_b16 v[80:81], %[wp] offset:22528\n" \
  "ds_read_b64_tr_b16 v[82:83], %[wp] offset:23552\n" \
  "ds_read_b128 v[84:87], %[oa] offset:11264\n" \
  "s_waitcnt lgkmcnt(9)\n" \
  "v_mfma_f32_32x32x16_bf16 %[a1], v[88:91], v[92:95], %[a1]\n" \
  "ds_read_b64_tr_b16 v[88:89], %[wp] offset:55296\n" \
  "ds_read_b64_tr_b16 v[90:91], %[wp] offset:56320\n" \
  "ds_read_b128 v[92:95], %[ob] offset:11264\n" \
  "s_waitcnt lgkmcnt(9)\n" \
  "v_mfma_f32_32x32x16_bf16 %[a0], v[64:67], v[68:71], %[a0]\n" \
  "ds_read_b64_tr_b16 v[64:65], %[wp] offset:24576\n" \
  "ds_read_b64_tr_b16 v[66:67], %[wp] offset:25600\n" \
  "ds_read_b128 v[68:71], %[oa] offset:12288\n" \
  "s_waitcnt lgkmcnt(9)\n" \
  "v_mfma_f32_32x32x16_bf16 %[a1], v[72:75], v[76:79], %[a1]\n" \
  "ds_read_b64_tr_b16 v[72:73], %[wp] offset:57344\n" \
  "ds_read_b64_tr_b16 v[74:75], %[wp] offset:58368\n" \
  "ds_read_b128 v[76:79], %[ob] offset:12288\n" \
  "s_waitcnt lgkmcnt(9)\n" \
  "v_mfma_f32_32x32x16_bf16 %[a0], v[80:83], v[84:87], %[a0]\n" \
  "ds_read_b64_tr_b16 v[80:81], %[wp] offset:26624\n" \
  "ds_read_b64_tr_b16 v[82:83], %[wp] offset:27648\n" \
  "ds_read_b128 v[84:87], %[oa] offset:13312\n" \
  "s_waitcnt lgkmcnt(9)\n" \
  "v_mfma_f32_32x32x16_bf16 %[a1], v[88:91], v[92:95], %[a1]\n" \
  "ds_read_b64_tr_b16 v[88:89], %[wp] offset:59392\n" \
  "ds_read_b64_tr_b16 v[90:91], %[wp] offset:60416\n" \
  "ds_read_b128 v[92:95], %[ob] offset:13312\n" \
  "s_waitcnt lgkmcnt(9)\n" \
  "v_mfma_f32_32x32x16_bf16 %[a0], v[64:67], v[68:71], %[a0]\n" \
  "ds_read_b64_tr_b16 v[64:65], %[wp] offset:28672\n" \
  "ds_read_b64_tr_b16 v[66:67], %[wp] offset:29696\n" \
  "ds_read_b128 v[68:71], %[oa] offset:14336\n" \
  "s_waitcnt lgkmcnt(9)\n" \
  "v_mfma_f32_32x32x16_bf16 %[a1], v[72:75], v[76:79], %[a1]\n" \
  "ds_read_b64_tr_b16 v[72:73], %[wp] offset:61440\n" \
  "ds_read_b64_tr_b16 v[74:75], %[wp] offset:62464\n" \
  "ds_read_b128 v[76:79], %[ob] offset:14336\n" \
  "s_waitcnt lgkmcnt(9)\n" \
  "v_mfma_f32_32x32x16_bf16 %[a0], v[80:83], v[84:87], %[a0]\n" \
  "ds_read_b64_tr_b16 v[80:81], %[wp] offset:30720\n" \
  "ds_read_b64_tr_b16 v[82:83], %[wp] offset:31744\n" \
  "ds_read_b128 v[84:87], %[oa] offset:15360\n" \
  "s_waitcnt lgkmcnt(9)\n" \
  "v_mfma_f32_32x32x16_bf16 %[a1], v[88:91], v[92:95], %[a1]\n" \
  "ds_read_b64_tr_b16 v[88:89], %[wp] offset:63488\n" \
  "ds_read_b64_tr_b16 v[90:91], %[wp] offset:64512\n" \
  "ds_read_b128 v[92:95], %[ob] offset:15360\n" \
  "s_waitcnt lgkmcnt(9)\n" \
  "v_mfma_f32_32x32x16_bf16 %[a0], v[64:67], v[68:71], %[a0]\n" \
  "s_waitcnt lgkmcnt(6)\n" \
  "v_mfma_f32_32x32x16_bf16 %[a1], v[72:75], v[76:79], %[a1]\n" \
  "s_waitcnt lgkmcnt(3)\n" \
  "v_mfma_f32_32x32x16_bf16 %[a0], v[80:83], v[84:87], %[a0]\n" \
  "s_waitcnt lgkmcnt(0)\n" \
  "v_mfma_f32_32x32x16_bf16 %[a1], v[88:91], v[92:95], %[a1]\n" \
  "s_nop 15\n" \
  "s_nop 7\n"
#define OUT_LOOP_CLOBBERS "v64", "v65", "v66", "v67", "v68", "v69", "v70", "v71", "v72", "v73", "v74", "v75", "v76", "v77", "v78", "v79", "v80", "v81", "v82", "v83", "v84", "v85", "v86", "v87", "v88", "v89", "v90", "v91", "v92", "v93", "v94", "v95"
constexpr int AT_K = 0, AT_V = 65536;
constexpr int CV_H = 131072, CV_HB = 8320, CV_U = CV_H + 2 * CV_HB, CV_UB = 95 * 80  , LDS_BYTES = 163104;
constexpr int P3_WO = 49152;

__device__ __forceinline__ void issue_wo(const Params& p, LAS char* lds, int wid, int lane) {
    const int ct = (blockIdx.x >> 3) & 7;
    const srd_t sw = mksrd(p.wo, D * D * 2);
    const unsigned wv = (lane >> 3) * 1024 + ((((lane & 7) >> 2) ^ ((lane >> 4) & 1)) * 64) + (lane & 3) * 16 + ct * 128;
    const unsigned l0 = (unsigned)(uintptr_t)lds + P3_WO;
#pragma unroll
    for (int i = 0; i < 8; ++i) { const int m = wid * 8 + i; dma16(sw, wv, m * 8192, l0 + m * 1024); }
}

__device__ __forceinline__ void phase2(const Params& p, LAS char* lds, unsigned long long tag) {
    char* ws = p.ws;
    const int tid = threadIdx.x, lane = tid & 63;
    const int wid = __builtin_amdgcn_readfirstlane(tid >> 6);
    const int bid = blockIdx.x, xc = bid & 7, jj = bid >> 3;
    const int i32 = lane & 31, hh = lane >> 5;
    const srd_t srd = mksrd(ws, WS_BYTES);

    if (wid >= 8) {
        const int cw = wid - 8, c2 = cw >> 1, half = cw & 1, ch = (xc * 32 + jj) * 2 + c2;
        LAS char* hb = lds + CV_H + c2 * CV_HB;
        LAS char* ub = lds + CV_U + c2 * CV_UB;
        const int t128 = half * 64 + lane;
        const u16* hrow = p.ssk + (size_t)ch * S;
        const int head = xc * 4 + (jj >> 3), qb = jj & 7;
        lds_wait(lds, CNT_PUB, 1);
        if (wid == 8) {
            const unsigned fo = WS_F1A + (head * 8 + qb) * 256;
            const unsigned long long v = poll_block<24>(ws, fo, tag);
            float kn = decode12((unsigned)(v >> 12) & 0xfffu), qn = (((lane & 31) >> 2) == qb) ? decode12((unsigned)v & 0xfffu) : 0.f;
            kn = dppmax<0x143, 0xc>(dppmax<0x142, 0xa>(rowmax16(kn))); qn = dppmax<0x143, 0xc>(dppmax<0x142, 0xa>(rowmax16(qn)));
            if (lane == 63) *(LAS int*)(lds + FAST_LDS) = (kn * qn * 1.02f <= FAST_BOUND2) ? 1 : 0;
            WAITL0();
            lds_add(lds, CNT_GO1);
        } else lds_wait(lds, CNT_GO1, 1);

        {
            const unsigned voff = cw * 2048 + lane * 16, l0 = (unsigned)(uintptr_t)lds + cw * 2048, kb = WS_KF + head * 65536;
            unsigned t;
            asm volatile(KV_LOAD_ASM : [t] "=&s"(t) : [l0] "s"(l0), [kb] "s"(kb), [vo] "v"(voff), [srd] "s"(srd) : "scc", "memory");
            WAITV(24); lds_add(lds, CNT_KV);
            WAITV(16); lds_add(lds, CNT_KV + 4);
            WAITV(8); lds_add(lds, CNT_KV + 8);
            WAITV(0); lds_add(lds, CNT_KV + 12);
        }
        __builtin_amdgcn_s_setprio(CONV_PRIO);
        unsigned warm[2];
#pragma unroll
        for (int i = 0; i < 2; ++i) warm[i] = *(const GAS unsigned*)(p.wo + (size_t)((cw * 2 + i) * 64 + lane) * D + (jj & 7) * 64);
        u32x4 hv[2], uv[2]; unsigned prev[2];
#pragma unroll
        for (int i = 0; i < 2; ++i) { const int t = t128 + 128 * i;
            hv[i] = *(const u32x4*)(hrow + 8 * t); prev[i] = t > 0 ? (unsigned)hrow[8 * t - 1] : 0u; }
        const float dd = bflo((unsigned)p.ssd[ch]);

        asm volatile("" :: "v"(warm[0]), "v"(warm[1]));
        if (wid == 8) { (void)poll_block<0>(ws, WS_F1B + (xc * 32 + jj) * 256, tag); lds_add(lds, CNT_U); } else lds_wait<4>(lds, CNT_U, 1);
#pragma unroll
        for (int i = 0; i < 2; ++i) uv[i] = ld16(srd, WS_UR + (ch * S + 8 * (t128 + 128 * i)) * 2);
#pragma unroll
        for (int i = 0; i < 2; ++i) { const int t = t128 + 128 * i;
            *(LAS u32x4*)(hb + (16 + 4 * t) * 4) = hv[i];
            u32x4 sv = {(hv[i][0] << 16) | prev[i], __builtin_amdgcn_alignbit(hv[i][1], hv[i][0], 16), __builtin_amdgcn_alignbit(hv[i][2], hv[i][1], 16), __builtin_amdgcn_alignbit(hv[i][3], hv[i][2], 16)};
            *(LAS u32x4*)(hb + 4160 + (16 + 4 * t) * 4) = sv;
            *(LAS u32x4*)(ub + (t >> 2) * 80 + (t & 3) * 16) = uv[i]; }
        if (t128 < 16) { *(LAS unsigned*)(hb + t128 * 4) = 0u; *(LAS unsigned*)(hb + 4160 + t128 * 4) = 0u; }
        for (int i = t128; i < 31 * 5; i += 128) *(LAS u32x4*)(ub + 64 * 80 + i * 16) = u32x4{0u, 0u, 0u, 0u};
        WAITL0(); lds_add(lds, CNT_CB + c2 * 4); lds_wait(lds, CNT_CB + c2 * 4, 2);

        const int r = i32;
        f32x16 acc0 = {}, acc1 = {};
        __builtin_amdgcn_s_setprio(0);
        {
            const unsigned ap = (unsigned)(uintptr_t)hb + ((r & 1) ? 0 : 4160) + (((r + 2) >> 1) + 4 * hh) * 4 + half * 16 * 64;
            const unsigned bp = (unsigned)(uintptr_t)ub + (31 - r) * 80 + 16 * hh + half * 16 * 80;
            asm volatile(CONV_LOOP_ASM : [a0] "+v"(acc0), [a1] "+v"(acc1) : [ap] "v"(ap), [aq] "v"(ap + 2048), [bp] "v"(bp) : CONV_LOOP_CLOBBERS, "memory");
        }

        __builtin_amdgcn_s_setprio(CONV_PRIO);
        lds_add(lds, CNT_CDONE + c2 * 4);
        {
            lds_wait(lds, CNT_CDONE, 2); lds_wait(lds, CNT_CDONE + 4, 2);
            const f32x16& snd = half ? acc0 : acc1;
            LAS char* px = lds + CV_H + cw * 4096 + lane * 16;
#pragma unroll
            for (int g = 0; g < 4; ++g) *(LAS f32x4*)(px + g * 1024) = f32x4{snd[4 * g], snd[4 * g + 1], snd[4 * g + 2], snd[4 * g + 3]};
        }
        WAITL0(); lds_add(lds, CNT_CPART + c2 * 4); lds_wait(lds, CNT_CPART + c2 * 4, 2);
        {
            const f32x16& own = half ? acc1 : acc0;
            LAS const char* px = lds + CV_H + (cw ^ 1) * 4096 + lane * 16;
            const int t0 = half * 1024;
            LAS const char* ue = ub + (63 - half * 32 - r) * 80;
            LAS char* zl = lds + CV_H + (cw ^ 1) * 4096;
            f32x4 po[4];
#pragma unroll
            for (int g = 0; g < 4; ++g) po[g] = *(LAS const f32x4*)(px + g * 1024);
            WAITL0();
#pragma unroll
            for (int g = 0; g < 4; ++g) {
                const f32x4 o = po[g];
                const int rr = 8 * g + 4 * hh;
                const u32x2 uu = *(LAS const u32x2*)(ue + (28 - rr) * 2);
                const float u3 = bflo(uu[0]), u2 = bfhi(uu[0]), u1 = bflo(uu[1]), u0 = bfhi(uu[1]);
                const float z0 = rbf(own[4 * g + 0] + o[0]) + rbf(u0 * dd), z1 = rbf(own[4 * g + 1] + o[1]) + rbf(u1 * dd);
                const float z2 = rbf(own[4 * g + 2] + o[2]) + rbf(u2 * dd), z3 = rbf(own[4 * g + 3] + o[3]) + rbf(u3 * dd);
                *(LAS u32x2*)(zl + (32 * r + rr) * 2) = u32x2{cvtpk(z0, z1), cvtpk(z2, z3)};
            }
            WAITL0();
#pragma unroll
            for (int i = 0; i < 2; ++i) st16(srd, WS_ZT + (ch * S + t0) * 2 + i * 1024 + lane * 16, *(LAS const u32x4*)(zl + i * 1024 + lane * 16));
        }

        WAITV(0);
        if (lds_inc(lds, CNT_Z) == 3 && lane < 32) put_flag(ws, WS_F2B + ((xc * 32 + lane) * 32 + jj) * 8, tag);
        return;
    }
    const int head = xc * 4 + (jj >> 3), qb = jj & 7;
    const int qp = wid & 3, kh = wid >> 2;
    const int qt = qb * 8 + 2 * qp;
    lds_wait(lds, CNT_GO1, 1);

    const bool fast = __builtin_amdgcn_readfirstlane(*(LAS const int*)(lds + FAST_LDS)) != 0;
    const unsigned fill = (i32 == 16 || i32 == 20) ? 0x3F803F80u : 0u;
    const unsigned oq = WS_QF + ((head * 64 + qt) * 64 + lane) * 16;
    f32x16 o1 = {}, o2 = {};

    if (fast) {
        unsigned ka = (unsigned)(uintptr_t)lds + AT_K + kh * 32768 + lane * 16;
        unsigned va = (unsigned)(uintptr_t)lds + AT_V + kh * 32768 + hh * 256 + (i32 & 15) * 16;
        unsigned g, t;
        const unsigned long long vm = 0x0000FFFF0000FFFFull;
        const unsigned cnt = (unsigned)(uintptr_t)lds + CNT_KV;
        asm volatile(ATTN_LOOP_ASM : [o1] "+v"(o1), [o2] "+v"(o2), [ka] "+v"(ka), [va] "+v"(va), [g] "=&s"(g), [t] "=&s"(t)
                     : [cnt] "v"(cnt), [qo] "v"(oq), [srd] "s"(srd), [vm] "s"(vm), [fill] "v"(fill), [sw] "s"(kh ? PRIO_GROUP : 99)
                     : ATTN_LOOP_CLOBBERS, "scc", "memory");
    } else {
        const bf16x8 q1 = __builtin_bit_cast(bf16x8, ld16(srd, oq)), q2 = __builtin_bit_cast(bf16x8, ld16(srd, oq + 1024));
        lds_wait(lds, CNT_KV, 4); lds_wait(lds, CNT_KV + 4, 4); lds_wait(lds, CNT_KV + 8, 4); lds_wait(lds, CNT_KV + 12, 4);
        const bool real = i32 < 16;
        const bf16x8 fl = __builtin_bit_cast(bf16x8, u32x4{fill, fill, fill, fill});
        float m1 = -INFINITY, m2 = -INFINITY;
        for (int kt = 0; kt < 64; ++kt) {
            const bf16x8 kf = *(LAS const bf16x8*)(lds + AT_K + kt * 1024 + lane * 16);
            const f32x16 s1 = MFMA32(kf, q1, (f32x16){}), s2 = MFMA32(kf, q2, (f32x16){});
#pragma unroll
            for (int r = 0; r < 16; ++r) { m1 = __builtin_fmaxf(m1, s1[r]); m2 = __builtin_fmaxf(m2, s2[r]); }
        }
        m1 = __builtin_fmaxf(m1, swap32(m1)); m2 = __builtin_fmaxf(m2, swap32(m2));
        if (m1 == -INFINITY) m1 = 0.f;
        if (m2 == -INFINITY) m2 = 0.f;
        for (int kt = kh * 32; kt < kh * 32 + 32; ++kt) {
            const bf16x8 kf = *(LAS const bf16x8*)(lds + AT_K + kt * 1024 + lane * 16);
            bf16x8 v0 = *(LAS const bf16x8*)(lds + AT_V + kt * 1024 + hh * 256 + (i32 & 15) * 16);
            bf16x8 v1 = *(LAS const bf16x8*)(lds + AT_V + kt * 1024 + 512 + hh * 256 + (i32 & 15) * 16);
            v0 = real ? v0 : fl; v1 = real ? v1 : fl;
            f32x16 s1 = MFMA32(kf, q1, (f32x16){}), s2 = MFMA32(kf, q2, (f32x16){});
#pragma unroll
            for (int r = 0; r < 16; ++r) { s1[r] = __builtin_amdgcn_exp2f(s1[r] - m1); s2[r] = __builtin_amdgcn_exp2f(s2[r] - m2); }
            o1 = smf(v0, lo8(s1), o1); o1 = smf(v1, hi8(s1), o1);
            o2 = smf(v0, lo8(s2), o2); o2 = smf(v1, hi8(s2), o2);
        }
    }

    lds_add(lds, CNT_ADONE); lds_wait(lds, CNT_ADONE, 8);

    {
        const f32x16& snd = kh ? o1 : o2;
        LAS char* px = lds + wid * 4096 + lane * 16;
        *(LAS f32x4*)(px) = f32x4{snd[0], snd[1], snd[2], snd[3]};
        *(LAS f32x4*)(px + 1024) = f32x4{snd[4], snd[5], snd[6], snd[7]};
        *(LAS float*)(lds + wid * 4096 + 2048 + lane * 4) = snd[8];
    }
    WAITL0(); lds_add(lds, CNT_APART); lds_wait(lds, CNT_APART, 8);

    {
        const f32x16& own = kh ? o2 : o1;
        LAS const char* px = lds + (wid ^ 4) * 4096 + lane * 16;
        const f32x4 a = *(LAS const f32x4*)(px), b = *(LAS const f32x4*)(px + 1024);
        const float l = own[8] + *(LAS const float*)(lds + (wid ^ 4) * 4096 + 2048 + lane * 4);
        const float inv = 1.0f / l;
        st16(srd, WS_OF + (((qt + kh) * 32 + head) * 64 + lane) * 16,
             u32x4{cvtpk((own[0] + a[0]) * inv, (own[1] + a[1]) * inv), cvtpk((own[2] + a[2]) * inv, (own[3] + a[3]) * inv),
                   cvtpk((own[4] + b[0]) * inv, (own[5] + b[1]) * inv), cvtpk((own[6] + b[2]) * inv, (own[7] + b[3]) * inv)});
    }

    WAITV(0);
    if (lds_inc(lds, CNT_O) == 7 && lane < 32) put_flag(ws, WS_F2A + ((qb * 32 + lane) * 32 + head) * 8, tag);
    issue_wo(p, lds, wid, lane);
}

__device__ __forceinline__ void phase3(const Params& p, LAS char* lds, unsigned long long tag) {
    char* ws = p.ws;
    const int tid = threadIdx.x, lane = tid & 63;
    const int wid = __builtin_amdgcn_readfirstlane(tid >> 6);
    const int bid = blockIdx.x, xc = bid & 7, jj = bid >> 3;
    const srd_t srd = mksrd(ws, WS_BYTES);
    const int rt = xc * 4 + (jj >> 3), ct = jj & 7;

    if (wid >= 8) {
        const int cw = wid - 8, st = rt, d0 = ct * 64 + cw * 16;
        LAS char* tl = lds + CV_H + (cw ^ 1) * 4096;
        u32x4 z[2], qv[2];
#pragma unroll
        for (int i = 0; i < 2; ++i) { const int e = lane + 64 * i; qv[i] = *(const u32x4*)(p.q + (size_t)(st * 64 + (e >> 1)) * D + d0 + (e & 1) * 8); }
        if (wid == 8) { (void)poll_block<0>(ws, WS_F2B + (ct * 32 + rt) * 256, tag); lds_add(lds, CNT_ZGO); } else lds_wait(lds, CNT_ZGO, 1);
#pragma unroll
        for (int i = 0; i < 2; ++i) { const int e = lane + 64 * i; z[i] = ld16(srd, WS_ZT + ((d0 + (e >> 3)) * S + st * 64 + (e & 7) * 8) * 2); }
#pragma unroll
        for (int i = 0; i < 2; ++i) { const int e = lane + 64 * i; *(LAS u32x4*)(tl + (e >> 3) * 144 + (e & 7) * 16) = z[i]; }
        WAITL0();
#pragma unroll
        for (int i = 0; i < 2; ++i) { const int e = lane + 64 * i, sl = e >> 1, c = (e & 1) * 8;
            u32x4 ov;
#pragma unroll
            for (int j = 0; j < 4; ++j) { const float z0 = bflo((unsigned)*(LAS const u16*)(tl + (c + 2 * j) * 144 + sl * 2)), z1 = bflo((unsigned)*(LAS const u16*)(tl + (c + 2 * j + 1) * 144 + sl * 2));
                ov[j] = cvtpk(bflo(qv[i][j]) * z0, bfhi(qv[i][j]) * z1); }
            __builtin_amdgcn_raw_buffer_store_b128(ov, mksrd(p.out, S * 2 * D * 2), ((st * 64 + sl) * (2 * D) + d0 + c) * 2, 0, 0); }

        return;
    }
    if (wid == 0) {
        lds_wait(lds, CNT_O, 8);
        (void)poll_block<0>(ws, WS_F2A + ((rt >> 2) * 32 + (rt & 3) * 8 + ct) * 256, tag);
        lds_add(lds, CNT_GO);
        if (bid == 0 && lane == 0) put_flag(ws, WS_EPOCH, tag);
    } else lds_wait(lds, CNT_GO, 1);
    {
        const unsigned l0 = (unsigned)(uintptr_t)lds;
#pragma unroll
        for (int i = 0; i < 8; ++i) { const int m = wid * 8 + i;
            dma16c(srd, lane * 16, WS_OF + (rt * 2 * 32 + m) * 1024, l0 + (m < 48 ? m * 1024 : 114688 + (m - 48) * 1024)); }
    }
    WAITV(0);
    lds_add(lds, CNT_LAND);
    if (wid >= 4) return;
    lds_wait(lds, CNT_LAND, 8);

    const int wr = wid >> 1, wc = wid & 1, hh = lane >> 5, qrow = (lane & 15) >> 2;
    LAS const char* wp = lds + P3_WO + (4 * hh + qrow) * 128 + ((wc ^ (qrow >> 1)) * 64) + ((lane >> 4) & 1) * 32 + (lane & 3) * 8;
    LAS const char* opa = lds + wr * 32768 + lane * 16;
    LAS const char* opb = lds + (wr ? 114688 : 16384) + lane * 16;
    f32x16 acc = {}, acc2 = {};
    asm volatile(OUT_LOOP_ASM : [a0] "+v"(acc), [a1] "+v"(acc2) : [wp] "v"((unsigned)(uintptr_t)wp), [oa] "v"((unsigned)(uintptr_t)opa), [ob] "v"((unsigned)(uintptr_t)opb) : OUT_LOOP_CLOBBERS, "memory");

    unsigned pk[8];
#pragma unroll
    for (int j = 0; j < 8; ++j) pk[j] = cvtpk(acc[2 * j] + acc2[2 * j], acc[2 * j + 1] + acc2[2 * j + 1]);
    const unsigned orow = (((rt * 2 + wr) * 32 + (lane & 31)) * (2 * D) + D + ct * 64 + wc * 32 + hh * 8) * 2;
    const srd_t sout = mksrd(p.out, S * 2 * D * 2);
#pragma unroll
    for (int g = 0; g < 4; g += 2) {
        auto s0 = __builtin_amdgcn_permlane32_swap(pk[2 * g], pk[2 * g + 2], false, false);
        auto s1 = __builtin_amdgcn_permlane32_swap(pk[2 * g + 1], pk[2 * g + 3], false, false);
        __builtin_amdgcn_raw_buffer_store_b128(u32x4{s0[0], s1[0], s0[1], s1[1]}, sout, orow + g * 16, 0, 0);
    }

}

__global__ __launch_bounds__(768, 3) void concat_heads__ssm_conv_h3__softmax_full_mha__prompt_kernel(Params p) {
    extern __shared__ __attribute__((aligned(16))) char smem[];
    asm volatile("" :: "s"(p.q), "s"(p.k), "s"(p.v), "s"(p.shk), "s"(p.ssk), "s"(p.ssd), "s"(p.x), "s"(p.wqkv), "s"(p.wo), "s"(p.out), "s"(p.ws));
    unsigned long long tag = 0;
    if (threadIdx.x < N_CNT) *(LAS unsigned*)((LAS char*)smem + FAST_LDS + threadIdx.x * 4) = 0u;
    phase1(p, (LAS char*)smem, tag);
    phase2(p, (LAS char*)smem, tag);
    phase3(p, (LAS char*)smem, tag);
}

extern "C" void kernel_launch(void* const* d_in, const int* in_sizes, int n_in, void* d_out, int out_size, void* d_ws, size_t ws_size, hipStream_t stream) {
    Params p;
    p.q = (const u16*)d_in[0]; p.k = (const u16*)d_in[1]; p.v = (const u16*)d_in[2];
    p.shk = (const u16*)d_in[3]; p.ssk = (const u16*)d_in[4]; p.ssd = (const u16*)d_in[5];
    p.x = (const u16*)d_in[6]; p.wqkv = (const u16*)d_in[7]; p.wo = (const u16*)d_in[8];
    p.out = (u16*)d_out; p.ws = (char*)d_ws;
    (void)hipFuncSetAttribute((const void*)concat_heads__ssm_conv_h3__softmax_full_mha__prompt_kernel, hipFuncAttributeMaxDynamicSharedMemorySize, LDS_BYTES);
    concat_heads__ssm_conv_h3__softmax_full_mha__prompt_kernel<<<256, 768, LDS_BYTES, stream>>>(p);
}
```
